```python
import torch

def _softmax(x, axis=-1):
    return torch.softmax(x, dim=axis)
def _gelu(x):
    return torch.nn.functional.gelu(x, approximate="tanh")
def _silu(x):
    return torch.nn.functional.silu(x)
def _sigmoid(x):
    return torch.sigmoid(x)
def _layernorm(x, axis=-1, eps=1e-5):
    m = x.mean(dim=axis, keepdim=True)
    v = x.var(dim=axis, keepdim=True, unbiased=False)
    return (x - m) / torch.sqrt(v + eps)
def _scatter_add(tgt, idx, src, axis=0):
    return tgt.clone().index_add_(axis, idx, src)
def _segment_sum(src, seg, n):
    out = torch.zeros((n,) + src.shape[1:], dtype=src.dtype)
    return out.index_add_(0, seg, src)
def _pad(x, pw):
    flat = tuple(v for pair in reversed(pw) for v in pair)
    return torch.nn.functional.pad(x, flat)

kernel_name = 'sequential_preLN__softmax_full_mha__dense_activation_silu__prompt'

def setup_inputs():

    _w = (lambda a: (lambda t: t.to(torch.bfloat16) if t.is_floating_point() else t)(torch.as_tensor(a)))
    def _A_setup():
        B, S, D = 1, 2048, 448
        return {
            "x": _w((lambda *s: torch.randn(*s, dtype=torch.bfloat16) * 0.05)(B, S, D)),
            "wqkv": _w((lambda *s: torch.randn(*s, dtype=torch.bfloat16) * 0.05)(D, 3 * D)),
            "wo": _w((lambda *s: torch.randn(*s, dtype=torch.bfloat16) * 0.05)(D, D)),
        }
    _A_in = _A_setup()

    def _B_setup():
        B, S, D, F = 1, 2048, 448, 1792
        return {
            "x": _w((lambda *s: torch.randn(*s, dtype=torch.bfloat16) * 0.05)(B, S, D)),
            "w1": _w((lambda *s: torch.randn(*s, dtype=torch.bfloat16) * 0.8)(D, F) * (D ** -0.5)),
            "w2": _w((lambda *s: torch.randn(*s, dtype=torch.bfloat16) * 0.2)(F, D) * (F ** -0.5)),
        }
    _B_in = _B_setup()

    return {
        "A_x": _A_in["x"],
        "A_wqkv": _A_in["wqkv"],
        "A_wo": _A_in["wo"],
        "B_x": _B_in["x"],
        "B_w1": _B_in["w1"],
        "B_w2": _B_in["w2"],
    }

def reference(A_x, A_wqkv, A_wo, B_x, B_w1, B_w2):
    def _A_ref(x, wqkv, wo):
        B, S, D = x.shape
        H = 16; Dh = D // H
        qkv = (lambda x, *d: x.permute(*d))((x @ wqkv).reshape(B, S, 3, H, Dh), 2, 0, 3, 1, 4)
        q, k, v = qkv[0], qkv[1], qkv[2]
        scale = Dh ** -0.5
        scores = torch.einsum("bhqd,bhkd->bhqk", q, k) * scale
        p = _softmax(scores, axis=-1)
        o = torch.einsum("bhqk,bhkd->bhqd", p, v)
        return (lambda x, *d: x.permute(*d))(o, 0, 2, 1, 3).reshape(B, S, D) @ wo

    def _B_ref(x, w1, w2):
        h = torch.einsum("bsd,df->bsf", x, w1)
        h = _silu(h)
        out = torch.einsum("bsf,fd->bsd", h, w2)
        return out

    x = A_x
    h = x + _A_ref(_layernorm(x, axis=-1), A_wqkv, A_wo)
    h2 = h + _B_ref(_layernorm(h, axis=-1), B_w1, B_w2)
    return h2
```

```cpp
#include <hip/hip_runtime.h>
#include <hip/hip_bf16.h>
typedef __hip_bfloat16 bf16;
constexpr int S = 2048, D = 448, H = 16, DH = 28, F = 1792;
__device__ inline float b2f(bf16 x) { return __bfloat162float(x); }
__global__ void k_ln(const float* x, float* y) {
    int r = blockIdx.x; __shared__ float red[2][64];
    float s = 0, q = 0;
    for (int i = threadIdx.x; i < D; i += 64) { float v = x[r * D + i]; s += v; }
    for (int o = 32; o; o >>= 1) s += __shfl_xor(s, o);
    float m = s / D;
    for (int i = threadIdx.x; i < D; i += 64) { float v = x[r * D + i] - m; q += v * v; }
    for (int o = 32; o; o >>= 1) q += __shfl_xor(q, o);
    float rs = rsqrtf(q / D + 1e-5f);
    for (int i = threadIdx.x; i < D; i += 64) y[r * D + i] = (x[r * D + i] - m) * rs;
}
__global__ void k_cvt(const bf16* x, float* y, int n) { int i = blockIdx.x * 256 + threadIdx.x; if (i < n) y[i] = b2f(x[i]); }
__global__ void k_gemm(const float* A, const bf16* W, float* C, const float* res, int N, int K, int act, bf16* outb) {
    int n = blockIdx.x * 64 + threadIdx.x; int m = blockIdx.y;
    if (n >= N) return;
    float a = 0;
    for (int k = 0; k < K; ++k) a += A[m * K + k] * b2f(W[k * N + n]);
    if (act) a = a / (1.f + __expf(-a));
    if (res) a += res[m * N + n];
    if (outb) outb[m * N + n] = __float2bfloat16(a); else C[m * N + n] = a;
}
__global__ void k_attn(const float* qkv, float* o) {
    int q = blockIdx.x, h = blockIdx.y, lane = threadIdx.x;
    float qv[DH]; for (int d = 0; d < DH; ++d) qv[d] = qkv[q * 3 * D + h * DH + d] * 0.18898223650461362f;
    float m = -1e30f, l = 0, acc[DH]; for (int d = 0; d < DH; ++d) acc[d] = 0;
    for (int k = lane; k < S; k += 64) {
        float s = 0; for (int d = 0; d < DH; ++d) s += qv[d] * qkv[k * 3 * D + D + h * DH + d];
        float mn = fmaxf(m, s), al = __expf(m - mn), p = __expf(s - mn);
        l = l * al + p; for (int d = 0; d < DH; ++d) acc[d] = acc[d] * al + p * qkv[k * 3 * D + 2 * D + h * DH + d];
        m = mn;
    }
    float M = m; for (int of = 32; of; of >>= 1) M = fmaxf(M, __shfl_xor(M, of));
    float sc = __expf(m - M); l *= sc; for (int of = 32; of; of >>= 1) l += __shfl_xor(l, of);
    for (int d = 0; d < DH; ++d) { float a = acc[d] * sc; for (int of = 32; of; of >>= 1) a += __shfl_xor(a, of); if (lane == 0) o[q * D + h * DH + d] = a / l; }
}
extern "C" void kernel_launch(void* const* d_in, const int* in_sizes, int n_in, void* d_out, int out_size, void* d_ws, size_t ws_size, hipStream_t stream) {
    const bf16* x = (const bf16*)d_in[0]; const bf16* wqkv = (const bf16*)d_in[1]; const bf16* wo = (const bf16*)d_in[2];
    const bf16* w1 = (const bf16*)d_in[4]; const bf16* w2 = (const bf16*)d_in[5];
    float* ws = (float*)d_ws;
    float* xf = ws; float* ln = xf + S * D; float* qkv = ln + S * D; float* o = qkv + S * 3 * D; float* h = o + S * D; float* h1 = h + S * D;
    k_cvt<<<S * D / 256, 256, 0, stream>>>(x, xf, S * D);
    k_ln<<<S, 64, 0, stream>>>(xf, ln);
    k_gemm<<<dim3(21, S), 64, 0, stream>>>(ln, wqkv, qkv, nullptr, 3 * D, D, 0, nullptr);
    k_attn<<<dim3(S, H), 64, 0, stream>>>(qkv, o);
    k_gemm<<<dim3(7, S), 64, 0, stream>>>(o, wo, h, xf, D, D, 0, nullptr);
    k_ln<<<S, 64, 0, stream>>>(h, ln);
    k_gemm<<<dim3(28, S), 64, 0, stream>>>(ln, w1, h1, nullptr, F, D, 1, nullptr);
    k_gemm<<<dim3(7, S), 64, 0, stream>>>(h1, w2, nullptr, h, D, F, 0, (bf16*)d_out);
}
```

```cpp
#include <hip/hip_runtime.h>
#include <stdint.h>

typedef __attribute__((ext_vector_type(8))) __bf16 bf16x8;
typedef __attribute__((ext_vector_type(2))) __bf16 bf16x2;
typedef __attribute__((ext_vector_type(4))) short s16x4;
typedef __attribute__((ext_vector_type(16))) float f32x16;
typedef __attribute__((ext_vector_type(4))) float f32x4;
typedef __attribute__((ext_vector_type(2))) float f32x2;
typedef __attribute__((ext_vector_type(4))) unsigned u32x4;
typedef __attribute__((ext_vector_type(2))) unsigned u32x2;
typedef unsigned long long u64;
#define LAS __attribute__((address_space(3)))
#define GAS __attribute__((address_space(1)))
#define DI __device__ __forceinline__
typedef __amdgpu_buffer_rsrc_t rsrc_t;

constexpr int SEQ = 2048, DM = 448, NH = 16, DH = 28, FF = 1792;
constexpr unsigned WS_EPOCH = 0;
constexpr unsigned WS_F1 = 4096, WS_F2 = WS_F1 + 16 * 256, WS_F3 = WS_F2 + 16 * 256, WS_F4 = WS_F3 + 32 * 256;
constexpr unsigned WS_FS = WS_F4 + 32 * 256;
constexpr unsigned WS_CS1 = 32768;
constexpr unsigned WS_CS0 = WS_CS1 + 8192;
constexpr unsigned WS_XS = WS_CS0 + 8192;
constexpr unsigned WS_STAMP = 32u << 20;
constexpr unsigned WS_Q = 131072;
constexpr unsigned WS_K = WS_Q + 64 * 28 * 1024;
constexpr unsigned WS_V = WS_K + 64 * 28 * 1024;
constexpr unsigned WS_O = WS_V + 16 * 64 * 2048;
constexpr unsigned WS_H = WS_O + 64 * 28 * 1024;
constexpr unsigned WS_ST = WS_H + 64 * 28 * 1024;
constexpr unsigned WS_H1 = WS_ST + 64 * 28 * 256;
constexpr unsigned WS_END = WS_H1 + 64 * 112 * 1024;
constexpr unsigned KB = 1024;
constexpr unsigned L_XR = 0, L_WR = 64 * KB, L_CS = 148 * KB, L_XS = L_CS + 512, L_VT = 150 * KB;
constexpr unsigned L_A = 0, L_RING = 56 * KB, L_M = 152 * KB;
constexpr unsigned L_SCR = L_RING + 6 * 8 * KB;
constexpr unsigned L_BAD = 159 * KB;
constexpr unsigned LDS_BYTES = 160 * KB;
constexpr unsigned SPIN_CAP = 1u << 22;

#define MFMA(a, b, c) __builtin_amdgcn_mfma_f32_32x32x16_bf16(__builtin_bit_cast(bf16x8, a), __builtin_bit_cast(bf16x8, b), c, 0, 0, 0)
#define BAR() asm volatile("s_waitcnt lgkmcnt(0)\n\ts_barrier" ::: "memory")
#define WAITV(n) asm volatile("s_waitcnt vmcnt(" #n ")" ::: "memory")
#define SB() __builtin_amdgcn_sched_barrier(0)
#define BARX() asm volatile("s_barrier" ::: "memory")
#define PIN(x) asm volatile("" : "+v"(x))
#define EX2(x) __builtin_amdgcn_exp2f(x)
#define RLX_AGENT __ATOMIC_RELAXED, __HIP_MEMORY_SCOPE_AGENT

#define STAMP(i) do {} while (0)

template <int N> DI void waitv() { asm volatile("s_waitcnt vmcnt(%0)" ::"n"(N) : "memory"); }
DI void dma(rsrc_t rs, unsigned voff, unsigned soff, unsigned lds) {
    asm volatile("s_mov_b32 m0, %0\n\ts_nop 3\n\tbuffer_load_dwordx4 %1, %2, %3 offen lds" ::"s"(lds), "v"(voff), "s"(rs), "s"(soff) : "memory");
}
DI void dma_sc1(rsrc_t rs, unsigned voff, unsigned soff, unsigned lds) {
    asm volatile("s_mov_b32 m0, %0\n\ts_nop 3\n\tbuffer_load_dwordx4 %1, %2, %3 offen sc1 lds" ::"s"(lds), "v"(voff), "s"(rs), "s"(soff) : "memory");
}
DI void dma_dep(rsrc_t rs, unsigned voff, unsigned soff, unsigned lds, u32x4 dep) {
    asm volatile("s_mov_b32 m0, %0\n\ts_nop 3\n\tbuffer_load_dwordx4 %1, %2, %3 offen lds" ::"s"(lds), "v"(voff), "s"(rs), "s"(soff), "v"(dep) : "memory");
}
DI u32x4 ld16(rsrc_t rs, unsigned voff, unsigned soff) { return __builtin_amdgcn_raw_buffer_load_b128(rs, voff, soff, 16); }
DI void st16(rsrc_t rs, u32x4 v, unsigned voff, unsigned soff) { __builtin_amdgcn_raw_buffer_store_b128(v, rs, voff + soff, 0, 16); }
DI void st8(rsrc_t rs, u32x2 v, unsigned voff, unsigned soff) { __builtin_amdgcn_raw_buffer_store_b64(v, rs, voff, soff, 16); }
DI u32x4 lds16(unsigned a) { return *(const LAS u32x4*)(uintptr_t)a; }
DI f32x4 lds16f(unsigned a) { return *(const LAS f32x4*)(uintptr_t)a; }
DI u32x2 ldstr(unsigned a) { return __builtin_bit_cast(u32x2, __builtin_amdgcn_ds_read_tr16_b64_v4i16((LAS s16x4*)(uintptr_t)a)); }
template <int D2> DI u32x4 wfrag(unsigned a) { u32x2 lo = ldstr(a), hi = ldstr(a + D2); return u32x4{lo[0], lo[1], hi[0], hi[1]}; }
DI unsigned pk(float lo, float hi) { return __builtin_bit_cast(unsigned, __builtin_convertvector(f32x2{lo, hi}, bf16x2)); }
DI float bflo(unsigned u) { return __uint_as_float(u << 16); }
DI float bfhi(unsigned u) { return __uint_as_float(u & 0xffff0000u); }
DI float swap32(float v) {
    auto r = __builtin_amdgcn_permlane32_swap(__float_as_uint(v), __float_as_uint(v), false, false);
    return (threadIdx.x & 32) ? __uint_as_float(r[0]) : __uint_as_float(r[1]);
}
DI float dot2(unsigned a, unsigned b, float c) { return __builtin_amdgcn_fdot2_f32_bf16(__builtin_bit_cast(bf16x2, a), __builtin_bit_cast(bf16x2, b), c, false); }

DI void wait_flags(char* ws, unsigned off, int n, u64 epoch, int lane) {
    const GAS u64* p = (const GAS u64*)(ws + off) + (lane < n ? lane : 0);
    for (unsigned sp = 0; sp < SPIN_CAP; ++sp) {
        u64 v = __hip_atomic_load(p, RLX_AGENT);
        if (__all(v == epoch)) break;
    }
    asm volatile("" ::: "memory");
}
DI void wait_flags2(char* ws, unsigned off0, unsigned off1, u64 epoch) {
    const GAS u64* p0 = (const GAS u64*)(ws + off0); const GAS u64* p1 = (const GAS u64*)(ws + off1);
    for (unsigned sp = 0; sp < SPIN_CAP; ++sp) {
        const u64 v0 = __hip_atomic_load(p0, RLX_AGENT), v1 = __hip_atomic_load(p1, RLX_AGENT);
        if (__all(v0 == epoch && v1 == epoch)) break;
    }
    asm volatile("" ::: "memory");
}
DI void set_flag(char* ws, unsigned off, u64 epoch) { __hip_atomic_store((GAS u64*)(ws + off), epoch, RLX_AGENT); }

__global__ void __launch_bounds__(512, 2)
block_kernel(const void* __restrict__ gx, const void* __restrict__ gwqkv, const void* __restrict__ gwo, const void* __restrict__ gw1,
             const void* __restrict__ gw2, void* __restrict__ gout, char* __restrict__ ws) {
    extern __shared__ __attribute__((aligned(16))) char smem[];
    const int tid = threadIdx.x, lane = tid & 63, r = lane & 31, hh = lane >> 5;
    const int w = __builtin_amdgcn_readfirstlane(tid >> 6);
    const int b = blockIdx.x, xcd = b & 7, bi = b >> 3;
    const rsrc_t rX = __builtin_amdgcn_make_buffer_rsrc((void*)gx, 0, SEQ * DM * 2, 0x00020000);
    const rsrc_t rWqkv = __builtin_amdgcn_make_buffer_rsrc((void*)gwqkv, 0, DM * 3 * DM * 2, 0x00020000);
    const rsrc_t rWo = __builtin_amdgcn_make_buffer_rsrc((void*)gwo, 0, DM * DM * 2, 0x00020000);
    const rsrc_t rW1 = __builtin_amdgcn_make_buffer_rsrc((void*)gw1, 0, DM * FF * 2, 0x00020000);
    const rsrc_t rW2 = __builtin_amdgcn_make_buffer_rsrc((void*)gw2, 0, FF * DM * 2, 0x00020000);
    const rsrc_t rOut = __builtin_amdgcn_make_buffer_rsrc(gout, 0, SEQ * DM * 2, 0x00020000);
    const rsrc_t rWs = __builtin_amdgcn_make_buffer_rsrc(ws, 0, WS_END, 0x00020000);
    const int tb1 = 4 * (xcd >> 1) + bi / 7, c1 = 7 * (xcd & 1) + bi % 7;
    const int h2 = 2 * xcd + (bi & 1), qb = bi >> 1;
    const int tb3 = 2 * xcd + (bi >> 4), j3 = bi & 15, mh = j3 & 1, cb = j3 >> 1;
    const bool act3 = j3 < 14;
    const unsigned trn = (unsigned)((8 * hh + ((lane & 15) >> 2)) * 64 + ((lane >> 4) & 1) * 32 + (lane & 3) * 8);
    const unsigned trp = (unsigned)((4 * hh + ((lane & 15) >> 2)) * 64 + ((lane >> 4) & 1) * 32 + (lane & 3) * 8);
    const unsigned v1 = (lane >> 2) * 3584 + (lane & 3) * 16;
    const unsigned v2 = (lane >> 2) * 896 + (lane & 3) * 16;
    const unsigned lo = lane * 16;
    STAMP(0);
    const u64 epoch = __hip_atomic_load((const GAS u64*)(ws + WS_EPOCH), RLX_AGENT) + 1;
    if (tid == 0) *(LAS unsigned*)(uintptr_t)L_BAD = 0;

#define LGK() asm volatile("s_waitcnt lgkmcnt(0)" ::: "memory")
    if (bi >= 28) {
        const int id = (bi - 28) * 8 + xcd;
        const int sub = lane & 7, rw = lane >> 3;
        const int j1 = id < 21 ? id + 11 : id - 21;
        u32x4 xr[7], wa[7];
#pragma unroll
        for (int i = 0; i < 7; ++i) xr[i] = __builtin_amdgcn_raw_buffer_load_b128(rX, (unsigned)((id * 64 + w * 8 + rw) * 896 + sub * 16 + i * 128), 0, 0);
#define COLLD(RS, STRIDE, COL) do { _Pragma("unroll") for (int i = 0; i < 7; ++i) wa[i] = __builtin_amdgcn_raw_buffer_load_b128(RS, (unsigned)((w * 56 + i * 8 + rw) * (STRIDE) + sub * 16), (unsigned)(COL), 0); } while (0)
#define COLSUM(DST, FLAG) do { float c[8] = {0.f, 0.f, 0.f, 0.f, 0.f, 0.f, 0.f, 0.f}; \
            _Pragma("unroll") for (int i = 0; i < 7; ++i) _Pragma("unroll") for (int e = 0; e < 4; ++e) { c[2 * e] += bflo(wa[i][e]); c[2 * e + 1] += bfhi(wa[i][e]); } \
            *(LAS f32x4*)(uintptr_t)(tid * 32) = f32x4{c[0], c[1], c[2], c[3]}; *(LAS f32x4*)(uintptr_t)(tid * 32 + 16) = f32x4{c[4], c[5], c[6], c[7]}; \
            BAR(); \
            if (w == 0) { float t = 0.f; _Pragma("unroll 8") for (int i = 0; i < 64; ++i) t += *(const LAS float*)(uintptr_t)(i * 256 + lane * 4); \
                __builtin_amdgcn_raw_buffer_store_b32(__float_as_uint(t), rWs, lane * 4, (DST), 16); WAITV(0); if (lane == 0) set_flag(ws, (FLAG), epoch); } \
            BAR(); } while (0)
        if (id < 21) COLLD(rWqkv, 2688, id * 128); else COLLD(rW1, 3584, j1 * 128);
        float s1 = 0.f, s2 = 0.f;
#pragma unroll
        for (int i = 0; i < 7; ++i)
#pragma unroll
            for (int e = 0; e < 4; ++e) { s1 = dot2(xr[i][e], 0x3F803F80u, s1); s2 = dot2(xr[i][e], xr[i][e], s2); }
#pragma unroll
        for (int o = 1; o < 8; o <<= 1) { s1 += __shfl_xor(s1, o); s2 += __shfl_xor(s2, o); }
        const float mean = s1 * (1.f / DM), rstd = __builtin_amdgcn_rsqf(fmaxf(s2 * (1.f / DM) - mean * mean, 0.f) + 1e-5f);
        if (sub == 0) st8(rWs, u32x2{__float_as_uint(mean), __float_as_uint(rstd)}, (unsigned)(w * 8 + rw) * 8, WS_XS + (unsigned)id * 512);
        WAITV(0);
        BAR();
        if (tid == 64) set_flag(ws, WS_FS + id * 8, epoch);
        if (id < 21) {
            COLSUM(WS_CS0 + (unsigned)id * 256, WS_FS + 256 + id * 8);
            if (id < 17) COLLD(rW1, 3584, j1 * 128);
        }
        if (id < 17 || id >= 21) COLSUM(WS_CS1 + (unsigned)j1 * 256, WS_FS + 512 + j1 * 8);
    } else {
      if (w < 4) {
        const int rin = lane >> 3, sl = lane & 7;
        const unsigned ve = rin * 896 + ((sl ^ (rin >> 1)) * 16), vo = rin * 896 + ((sl ^ (4 + (rin >> 1))) * 16);
        const unsigned xs = (unsigned)(tb1 * 128 + w * 32) * 896, xl = L_XR + w * 16 * KB;
#define XDMA(c) do { _Pragma("unroll") for (int g = 0; g < 4; ++g) dma(rX, (g & 1) ? vo : ve, xs + g * 8 * 896 + (c) * 128, xl + ((c) & 3) * 4 * KB + g * KB); } while (0)
        XDMA(0); XDMA(1); XDMA(2); XDMA(3);
        const unsigned xb = xl + (r >> 3) * KB + (r & 7) * 128 + ((hh ^ ((r >> 1) & 7)) * 16);
        u32x4 xf[28];
#define XRD(c) do { _Pragma("unroll") for (int s = 0; s < 4; ++s) xf[4 * (c) + s] = lds16((xb ^ (s * 32)) + ((c) & 3) * 4 * KB); } while (0)
        WAITV(12); XRD(0); LGK(); XDMA(4);
        WAITV(12); XRD(1); LGK(); XDMA(5);
        WAITV(12); XRD(2); LGK(); XDMA(6);
        WAITV(12); XRD(3);
        STAMP(10);
        f32x16 ac[3] = {{}, {}, {}};
        unsigned wbA = L_WR + trn, wbB = L_WR + trn + 63 * KB;
        PIN(wbA); PIN(wbB);
        u32x4 fw[3][3];
#define WRD(ks) do { _Pragma("unroll") for (int t = 0; t < 3; ++t) fw[(ks) % 3][t] = (ks) < 21 ? wfrag<256>(wbA + ((ks) * 3 + t) * KB) : wfrag<256>(wbB + (((ks) - 21) * 3 + t) * KB); } while (0)
        BARX(); STAMP(11); WRD(0); WRD(1); SB();
#pragma unroll
        for (int ks = 0; ks < 28; ++ks) {
            ac[0] = MFMA(fw[ks % 3][0], xf[ks], ac[0]); PIN(ac[0]);
            SB();
            if (ks + 2 < 28) {
                if ((ks + 2) % 4 == 0) {
                    if (ks + 2 == 16) { WAITV(8); XRD(4); }
                    if (ks + 2 == 20) { WAITV(4); XRD(5); }
                    if (ks + 2 == 24) { WAITV(0); XRD(6); }
                    BARX();
                }
                WRD(ks + 2);
            }
            SB();
            ac[1] = MFMA(fw[ks % 3][1], xf[ks], ac[1]); PIN(ac[1]); ac[2] = MFMA(fw[ks % 3][2], xf[ks], ac[2]); PIN(ac[2]);
            SB();
        }
        STAMP(12);
        BAR();
        STAMP(13);
        const f32x2 xs2 = *(const LAS f32x2*)(uintptr_t)(L_XS + (w * 32 + r) * 8);
        const float mean = xs2[0], rstd = xs2[1];
        const unsigned kt = (unsigned)(tb1 * 4 + w);
#pragma unroll
        for (int t = 0; t < 3; ++t) {
            const int nt = 3 * c1 + t, kind = nt / 14, nl = nt - 14 * kind;
            const float sc = kind == 0 ? rstd * (0.18898223650461362f * 1.4426950408889634f) : rstd;
            u32x2 pkd[4];
#pragma unroll
            for (int g = 0; g < 4; ++g) {
                const f32x4 c = lds16f(L_CS + t * 128 + (8 * g + 4 * hh) * 4);
                pkd[g] = u32x2{pk((ac[t][4 * g] - mean * c[0]) * sc, (ac[t][4 * g + 1] - mean * c[1]) * sc), pk((ac[t][4 * g + 2] - mean * c[2]) * sc, (ac[t][4 * g + 3] - mean * c[3]) * sc)};
            }
            if (kind < 2) {
                const unsigned base = (kind == 0 ? WS_Q : WS_K) + kt * 28672 + (unsigned)nl * 2048;
#pragma unroll
                for (int g = 0; g < 4; g += 2) {
                    const auto x0 = __builtin_amdgcn_permlane32_swap(pkd[g][0], pkd[g + 1][0], false, false), x1 = __builtin_amdgcn_permlane32_swap(pkd[g][1], pkd[g + 1][1], false, false);
                    st16(rWs, u32x4{x0[0], x1[0], x0[1], x1[1]}, lo + g * 512, base);
                }
            } else {
                const unsigned vt = L_VT + w * 2048;
#pragma unroll
                for (int g = 0; g < 4; ++g) *(LAS u32x2*)(uintptr_t)(vt + r * 64 + (2 * g + hh) * 8) = pkd[g];
                LGK();
                const u32x4 f0 = wfrag<512>(vt + trp), f1 = wfrag<512>(vt + trp + 1024);
                const int n = nl * 32 + r, hd = (n * 2341) >> 16, d = n - 28 * hd;
                const unsigned off = (unsigned)hd * (64 * 2048) + (unsigned)((hh * 32 + d) * 16);
                st16(rWs, f0, off, WS_V + kt * 2048); st16(rWs, f1, off + 1024, WS_V + kt * 2048);
                if (d == 27) {
                    const u32x4 one = {0x3F803F80u, 0x3F803F80u, 0x3F803F80u, 0x3F803F80u};
                    st16(rWs, one, off + 16, WS_V + kt * 2048); st16(rWs, one, off + 1024 + 16, WS_V + kt * 2048);
                }
                LGK();
            }
        }
        STAMP(14);
        WAITV(0);
      } else {
        const int j = w - 4;
        const unsigned vw = (lane >> 2) * 2688 + (lane & 3) * 16;
        const unsigned so = (unsigned)(j * 16 * 2688 + c1 * 192), ld = L_WR + j * 3 * KB;
#pragma unroll
        for (int st = 0; st < 7; ++st) {
            dma(rWqkv, vw, so + st * 64 * 2688, ld + st * 12 * KB); dma(rWqkv, vw, so + st * 64 * 2688 + 64, ld + st * 12 * KB + KB); dma(rWqkv, vw, so + st * 64 * 2688 + 128, ld + st * 12 * KB + 2 * KB);
        }
        waitv<18>(); BARX(); waitv<15>(); BARX(); waitv<12>(); BARX(); waitv<9>(); BARX(); waitv<6>(); BARX(); waitv<3>(); BARX(); waitv<0>(); BARX();
        if (j == 0) {
            wait_flags2(ws, WS_FS + (unsigned)(2 * tb1 + (lane & 1)) * 8, WS_FS + 256 + (unsigned)((c1 * 96 + 95 * (lane & 1)) >> 6) * 8, epoch);
            const u32x4 a = ld16(rWs, lo, WS_XS + (unsigned)tb1 * 1024), c = ld16(rWs, lo, WS_CS0 + (unsigned)c1 * 384);
            *(LAS u32x4*)(uintptr_t)(L_XS + lo) = a;
            if (lane < 24) *(LAS u32x4*)(uintptr_t)(L_CS + lo) = c;
        }
        BAR();
      }
      BAR();
      if (tid == 0) set_flag(ws, WS_F1 + c1 * 256 + tb1 * 8, epoch);
    }
    STAMP(1);
    if (w == 0) {
        const int c0 = (28 * h2) & ~7;
        const int col = lane < 32 ? 448 + c0 + 31 * ((lane >> 4) & 1) : 896 + 28 * h2 + 27 * ((lane >> 4) & 1);
        wait_flags2(ws, WS_F1 + (unsigned)(col / 96) * 256 + (lane & 15) * 8, WS_F1 + (unsigned)((c0 + 31 * (lane & 1)) / 96) * 256 + qb * 8, epoch);
    } else if (act3) {
#pragma unroll
        for (int i = 0; i < 4; ++i) { const int id = i * 7 + w - 1; dma(rWo, v2, (unsigned)(id * 16 * 896 + cb * 128), L_A + id * 2 * KB); dma(rWo, v2, (unsigned)(id * 16 * 896 + cb * 128 + 64), L_A + id * 2 * KB + KB); }
        if (w < 7) {
#pragma unroll
            for (int i = 0; i < 4; ++i) { const int id = (w - 1) * 4 + i; dma(rW1, v1, (unsigned)((w - 1) * 16 * 3584 + cb * 512 + i * 128), L_RING + id * 2 * KB); dma(rW1, v1, (unsigned)((w - 1) * 16 * 3584 + cb * 512 + i * 128 + 64), L_RING + id * 2 * KB + KB); }
        }
    }
    BARX();
    STAMP(2);

    {
        const int qh = w & 1, kq = w >> 1;
        const unsigned ch = (unsigned)((28 * h2) >> 3) * 512;
        const unsigned qso = WS_Q + (unsigned)(qb * 4 + qh * 2) * 28672 + ch;
        const rsrc_t rK = __builtin_amdgcn_make_buffer_rsrc(ws + WS_K + (unsigned)kq * 16 * 28672 + ch, 0, 15 * 28672 + 2048, 0x00020000);
        const rsrc_t rV = __builtin_amdgcn_make_buffer_rsrc(ws + WS_V + (unsigned)(h2 * 64 + kq * 16) * 2048, 0, 16 * 2048, 0x00020000);
        u32x4 qa0 = ld16(rWs, lo, qso), qa1 = ld16(rWs, lo + 1024, qso), qb0 = ld16(rWs, lo, qso + 28672), qb1 = ld16(rWs, lo + 1024, qso + 28672);
        const u32x4 rk0 = ld16(rWs, lo, WS_K + ch), rk1 = ld16(rWs, lo + 1024, WS_K + ch);
#pragma unroll 1
        for (int pass = 0; pass < 2; ++pass) {
            u32x4 kf[4][2], vf[4][2], pa[2], pb[2];
#pragma unroll
            for (int i = 0; i < 4; ++i) {
                kf[i][0] = ld16(rK, lo, i * 28672); kf[i][1] = ld16(rK, lo + 1024, i * 28672);
                if (i < 3) { vf[i][0] = ld16(rV, lo, i * 2048); vf[i][1] = ld16(rV, lo + 1024, i * 2048); }
            }
            SB();
            if (h2 & 1) { if (!hh) { qa0[0] = 0u; qa0[1] = 0u; qb0[0] = 0u; qb0[1] = 0u; } }
            else if (hh) { qa1[2] = 0u; qa1[3] = 0u; qb1[2] = 0u; qb1[3] = 0u; }
            float ma, mb;
            if (pass == 0) {
                f32x16 z = {};
                f32x16 sa = MFMA(rk0, qa0, z), sb = MFMA(rk0, qb0, z); sa = MFMA(rk1, qa1, sa); sb = MFMA(rk1, qb1, sb);
                ma = sa[0]; mb = sb[0];
#pragma unroll
                for (int i = 1; i < 16; ++i) { ma = fmaxf(ma, sa[i]); mb = fmaxf(mb, sb[i]); }
                ma = fmaxf(ma, swap32(ma)); mb = fmaxf(mb, swap32(mb));
            } else {
                f32x16 z = {};
                ma = -INFINITY; mb = -INFINITY;
#pragma unroll 1
                for (int t = 0; t < 16; ++t) {
                    const u32x4 k0 = ld16(rK, lo, t * 28672), k1 = ld16(rK, lo + 1024, t * 28672);
                    f32x16 sa = MFMA(k0, qa0, z), sb = MFMA(k0, qb0, z); sa = MFMA(k1, qa1, sa); sb = MFMA(k1, qb1, sb);
#pragma unroll
                    for (int i = 0; i < 16; ++i) { ma = fmaxf(ma, sa[i]); mb = fmaxf(mb, sb[i]); }
                }
                ma = fmaxf(ma, swap32(ma)); mb = fmaxf(mb, swap32(mb));
                *(LAS float*)(uintptr_t)(L_M + 2048 + ((kq * 2 + qh) * 2 + hh) * 128 + r * 4) = hh ? mb : ma;
                BAR();
#pragma unroll
                for (int i = 0; i < 4; ++i) { ma = fmaxf(ma, *(const LAS float*)(uintptr_t)(L_M + 2048 + ((i * 2 + qh) * 2) * 128 + r * 4)); mb = fmaxf(mb, *(const LAS float*)(uintptr_t)(L_M + 2048 + ((i * 2 + qh) * 2 + 1) * 128 + r * 4)); }
                ma += fabsf(ma) * 9.5367431640625e-07f; mb += fabsf(mb) * 9.5367431640625e-07f;
            }
            f32x16 ca, cbv, oa = {}, ob = {};
#pragma unroll
            for (int i = 0; i < 16; ++i) { ca[i] = -ma; cbv[i] = -mb; }
            PIN(ca); PIN(cbv);
            vf[3][0] = vf[3][1] = pb[0] = pb[1] = u32x4{0u, 0u, 0u, 0u};
            f32x16 sa = MFMA(kf[0][0], qa0, ca), sb = {}; sa = MFMA(kf[0][1], qa1, sa);
            PIN(sa); STAMP(15);
            SB();
#define GRP(MF, S, i, T, j, P) do { MF; SB(); \
            S[4 * (i)] = EX2(S[4 * (i)]); S[4 * (i) + 1] = EX2(S[4 * (i) + 1]); S[4 * (i) + 2] = EX2(S[4 * (i) + 2]); S[4 * (i) + 3] = EX2(S[4 * (i) + 3]); SB(); \
            P[(j) >> 1][((j) & 1) * 2] = pk(T[4 * (j)], T[4 * (j) + 1]); P[(j) >> 1][((j) & 1) * 2 + 1] = pk(T[4 * (j) + 2], T[4 * (j) + 3]); PIN(P[(j) >> 1]); PIN(S); SB(); } while (0)
#pragma unroll 1
            for (int tq = 0; tq < 4; ++tq) {
                const unsigned sk = (unsigned)tq * 4 * 28672, sv = (unsigned)tq * 4 * 2048;
#pragma unroll
                for (int u = 0; u < 4; ++u) {
                    GRP(ob = MFMA(vf[(u + 3) & 3][0], pb[0], ob), sa, 0, sb, 3, pb);
                    GRP(sb = MFMA(kf[u][0], qb0, cbv), sa, 1, sa, 0, pa);
                    GRP(sb = MFMA(kf[u][1], qb1, sb), sa, 2, sa, 1, pa);
                    GRP(ob = MFMA(vf[(u + 3) & 3][1], pb[1], ob), sa, 3, sa, 2, pa);
                    kf[u][0] = ld16(rK, lo, sk + (u + 4) * 28672); kf[u][1] = ld16(rK, lo + 1024, sk + (u + 4) * 28672);
                    vf[(u + 3) & 3][0] = ld16(rV, lo, sv + (u + 3) * 2048); vf[(u + 3) & 3][1] = ld16(rV, lo + 1024, sv + (u + 3) * 2048);
                    SB();
                    GRP(oa = MFMA(vf[u][0], pa[0], oa), sb, 0, sa, 3, pa);
                    GRP(sa = MFMA(kf[(u + 1) & 3][0], qa0, ca), sb, 1, sb, 0, pb);
                    GRP(sa = MFMA(kf[(u + 1) & 3][1], qa1, sa), sb, 2, sb, 1, pb);
                    GRP(oa = MFMA(vf[u][1], pa[1], oa), sb, 3, sb, 2, pb);
                }
            }
            pb[1][2] = pk(sb[12], sb[13]); pb[1][3] = pk(sb[14], sb[15]);
            ob = MFMA(vf[3][0], pb[0], ob); ob = MFMA(vf[3][1], pb[1], ob);
            PIN(ob); STAMP(16);
            if (hh) { *(LAS float*)(uintptr_t)(L_M + ((kq * 2 + qh) * 2) * 128 + r * 4) = oa[12]; *(LAS float*)(uintptr_t)(L_M + ((kq * 2 + qh) * 2 + 1) * 128 + r * 4) = ob[12]; }
#pragma unroll
            for (int p = 0; p < 4; ++p) {
                if (p != kq) {
                    const unsigned dst = L_SCR + (unsigned)((qh * 4 + p) * 3 + (kq > p ? kq - 1 : kq)) * 2048 + lo;
                    const f32x16& o = (p >> 1) ? ob : oa;
                    *(LAS f32x4*)(uintptr_t)dst = f32x4{o[8 * (p & 1)], o[8 * (p & 1) + 1], o[8 * (p & 1) + 2], o[8 * (p & 1) + 3]};
                    *(LAS f32x4*)(uintptr_t)(dst + 1024) = f32x4{o[8 * (p & 1) + 4], o[8 * (p & 1) + 5], o[8 * (p & 1) + 6], o[8 * (p & 1) + 7]};
                }
            }
            float o8[8];
#pragma unroll
            for (int i = 0; i < 8; ++i) { const float lo8 = (kq & 2) ? ob[i] : oa[i], hi8 = (kq & 2) ? ob[8 + i] : oa[8 + i]; o8[i] = (kq & 1) ? hi8 : lo8; }
            BAR();
            float l = 0.f;
#pragma unroll
            for (int i = 0; i < 4; ++i) l += *(const LAS float*)(uintptr_t)(L_M + ((i * 2 + qh) * 2 + (kq >> 1)) * 128 + r * 4);
#pragma unroll
            for (int i = 0; i < 3; ++i) {
                const unsigned src = L_SCR + (unsigned)((qh * 4 + kq) * 3 + i) * 2048 + lo;
                const f32x4 a = lds16f(src), c = lds16f(src + 1024);
#pragma unroll
                for (int e = 0; e < 4; ++e) { o8[e] += a[e]; o8[4 + e] += c[e]; }
            }
            bool bad = !(l > 9.0949470177e-13f && l < 1.2676506e30f);
            const float il = __builtin_amdgcn_rcpf(l);
#pragma unroll
            for (int i = 0; i < 8; ++i) { if (i < 4 || !((kq & 1) && hh)) bad |= !(fabsf(o8[i]) < 3.0e38f); o8[i] *= il; }
            if (pass == 0 && __any(bad)) *(LAS unsigned*)(uintptr_t)L_BAD = 1;
            const unsigned tt = (unsigned)(qb * 4 + qh * 2 + (kq >> 1));
#pragma unroll
            for (int i = 0; i < 2; ++i) {
                const int m = 4 * (kq & 1) + 2 * i + hh, g = 7 * h2 + m;
                if (m < 7) st8(rWs, u32x2{pk(o8[4 * i], o8[4 * i + 1]), pk(o8[4 * i + 2], o8[4 * i + 3])}, (unsigned)(((g >> 2) * 64 + (g & 1) * 32 + r) * 16 + ((g >> 1) & 1) * 8), WS_O + tt * 28 * 1024);
            }
            WAITV(0);
            BAR();
            if (*(const volatile LAS unsigned*)(uintptr_t)L_BAD == 0) break;
        }
    }
    STAMP(3);
    if (tid == 0) set_flag(ws, WS_F2 + qb * 256 + h2 * 8, epoch);
    if (!act3) {
        if (b == 14 * 8 && w == 0) {
            wait_flags2(ws, WS_F2 + (lane >> 4) * 256 + (lane & 15) * 8, WS_F2 + (4 + (lane >> 4)) * 256 + (lane & 15) * 8, epoch);
            wait_flags2(ws, WS_F2 + (8 + (lane >> 4)) * 256 + (lane & 15) * 8, WS_F2 + (12 + (lane >> 4)) * 256 + (lane & 15) * 8, epoch);
            if (lane == 0) set_flag(ws, WS_EPOCH, epoch);
        }
        return;
    }

    const int mt = w & 1, kq = w >> 1;
    const unsigned tt0 = (unsigned)(tb3 * 4 + mh * 2);
#define W1PAIR(ks, q, ri) do { const unsigned so_ = (unsigned)((ks) * 16 * 3584 + cb * 512 + (q) * 128), ld_ = L_RING + ((ri) * 8 + (q) * 2) * KB; dma(rW1, v1, so_, ld_); dma(rW1, v1, so_ + 64, ld_ + KB); } while (0)
#define W2PAIR(t, q, row) do { const unsigned so_ = (unsigned)(((q) * 28 + (t)) * 16 * 896 + cb * 128), ld_ = (row) + (q) * 2 * KB; dma(rW2, v2, so_, ld_); dma(rW2, v2, so_ + 64, ld_ + KB); } while (0)
    unsigned w1A = L_RING + trp + w * KB, w1B = w1A + 48 * KB;
    PIN(w1A); PIN(w1B);
#define W1RD(i) ((i) < 6 ? wfrag<512>(w1A + (i) * 8 * KB) : wfrag<512>(w1B + ((i) - 6) * 8 * KB))
    u32x4 fr[28];
    const unsigned xo = ((tt0 + mt) * 32 + r) * 896 + (cb * 64 + 16 * kq + 4 * hh) * 2;
    u32x2 xr0 = __builtin_amdgcn_raw_buffer_load_b64(rX, xo, 0, 0), xr1 = __builtin_amdgcn_raw_buffer_load_b64(rX, xo + 16, 0, 0);
#define RED_WRITE(A0, A1) do { _Pragma("unroll") for (int p = 0; p < 4; ++p) { if (p != kq) { \
            const unsigned dst = L_A + (unsigned)((mt * 4 + p) * 3 + (kq > p ? kq - 1 : kq)) * 2048 + lo; const f32x16& o = (p >> 1) ? A1 : A0; \
            *(LAS f32x4*)(uintptr_t)dst = f32x4{o[8 * (p & 1)], o[8 * (p & 1) + 1], o[8 * (p & 1) + 2], o[8 * (p & 1) + 3]}; \
            *(LAS f32x4*)(uintptr_t)(dst + 1024) = f32x4{o[8 * (p & 1) + 4], o[8 * (p & 1) + 5], o[8 * (p & 1) + 6], o[8 * (p & 1) + 7]}; } } \
        _Pragma("unroll") for (int i = 0; i < 8; ++i) { const float lo8 = (kq & 2) ? A1[i] : A0[i], hi8 = (kq & 2) ? A1[8 + i] : A0[8 + i]; o8[i] = (kq & 1) ? hi8 : lo8; } } while (0)
#define RED_READ() do { _Pragma("unroll") for (int i = 0; i < 3; ++i) { const unsigned src = L_A + (unsigned)((mt * 4 + kq) * 3 + i) * 2048 + lo; \
            const f32x4 a = lds16f(src), c = lds16f(src + 1024); _Pragma("unroll") for (int e = 0; e < 4; ++e) { o8[e] += a[e]; o8[4 + e] += c[e]; } } } while (0)
    u32x4 hres;
    {
        if (w >= 4) {
#pragma unroll
            for (int i = 0; i < 6; ++i) W1PAIR(6 + i, w - 4, 6 + i);
        }
#pragma unroll
        for (int i = 0; i < 6; ++i) { fr[i] = W1RD(i); PIN(fr[i]); }
        u32x4 fa[14];
        const unsigned wb = L_A + trp + kq * 14 * KB;
#pragma unroll
        for (int i = 0; i < 14; ++i) { fa[i] = wfrag<512>(wb + i * KB); PIN(fa[i]); }
        if (w == 0) {
            wait_flags2(ws, WS_F2 + tb3 * 256 + (lane & 15) * 8, WS_FS + 512 + (cb * 4 + (lane & 3)) * 8, epoch);
        }
        BAR();
        STAMP(4);
        u32x4 bf[7];
#pragma unroll
        for (int i = 0; i < 7; ++i) bf[i] = ld16(rWs, lo, WS_O + ((tt0 + mt) * 28 + 7 * kq + i) * 1024);
        SB();
#pragma unroll
        for (int i = 0; i < 7; ++i) PIN(bf[i]);
#pragma unroll
        for (int i = 0; i < 3; ++i) W1PAIR(12 + 2 * i + (w >> 2), w & 3, 2 * i + (w >> 2));
        f32x16 a0 = {}, a1 = {};
#pragma unroll
        for (int i = 0; i < 7; ++i) { a0 = MFMA(fa[2 * i], bf[i], a0); a1 = MFMA(fa[2 * i + 1], bf[i], a1); }
        float o8[8];
        RED_WRITE(a0, a1);
        BAR();
        RED_READ();
        hres = u32x4{pk(o8[0] + bflo(xr0[0]), o8[1] + bfhi(xr0[0])), pk(o8[2] + bflo(xr0[1]), o8[3] + bfhi(xr0[1])),
                     pk(o8[4] + bflo(xr1[0]), o8[5] + bfhi(xr1[0])), pk(o8[6] + bflo(xr1[1]), o8[7] + bfhi(xr1[1]))};
        st16(rWs, hres, lo, WS_H + ((tt0 + mt) * 28 + cb * 4 + kq) * 1024);
        float s1 = 0.f, s2 = 0.f;
#pragma unroll
        for (int e = 0; e < 4; ++e) { const float a = bflo(hres[e]), c = bfhi(hres[e]); s1 += a + c; s2 = fmaf(a, a, fmaf(c, c, s2)); }
        s1 += swap32(s1); s2 += swap32(s2);
        if (hh == 0) st8(rWs, u32x2{__float_as_uint(s1), __float_as_uint(s2)}, r * 8, WS_ST + ((tt0 + mt) * 28 + cb * 4 + kq) * 256);
        WAITV(0);
        BAR();
    }
    STAMP(5);
    float rstd2;
    {
        if (tid == 0) set_flag(ws, WS_F3 + (tb3 * 2 + mh) * 256 + cb * 8, epoch);
        f32x4 cs[4];
#pragma unroll
        for (int g = 0; g < 4; ++g) cs[g] = __builtin_bit_cast(f32x4, ld16(rWs, (8 * g + 4 * hh) * 4, WS_CS1 + (unsigned)(cb * 256 + w * 32) * 4));
#pragma unroll
        for (int i = 6; i < 18; ++i) fr[i] = W1RD(i % 12);
#pragma unroll
        for (int i = 6; i < 18; ++i) PIN(fr[i]);
#pragma unroll
        for (int g = 0; g < 4; ++g) PIN(cs[g]);
        BAR();
        if (w == 0) wait_flags(ws, WS_F3 + (tb3 * 2 + mh) * 256, 7, epoch, lane);
        else if (w >= 4) {
#pragma unroll
            for (int i = 0; i < 10; ++i) W1PAIR(18 + i, w - 4, i);
            W2PAIR(0, w - 4, L_RING + 80 * KB); W2PAIR(1, w - 4, L_RING + 88 * KB);
        }
        BARX();
        STAMP(6);
#pragma unroll
        for (int i = 0; i < 7; ++i) { const int p = 8 * i + w; dma_sc1(rWs, lo, WS_H + ((tt0 + (p & 1)) * 28 + (p >> 1)) * 1024, L_A + p * KB); }
        {
            u32x2 v[4];
#pragma unroll
            for (int i = 0; i < 4; ++i) v[i] = __builtin_amdgcn_raw_buffer_load_b64(rWs, (unsigned)(hh * 28 * 256 + r * 8), 8 * i + w < 28 ? WS_ST + (tt0 * 28 + 8 * i + w) * 256 : WS_END, 16);
            const float s1 = (__uint_as_float(v[0][0]) + __uint_as_float(v[1][0])) + (__uint_as_float(v[2][0]) + __uint_as_float(v[3][0]));
            const float s2 = (__uint_as_float(v[0][1]) + __uint_as_float(v[1][1])) + (__uint_as_float(v[2][1]) + __uint_as_float(v[3][1]));
            *(LAS f32x2*)(uintptr_t)(L_M + w * 512 + lane * 8) = f32x2{s1, s2};
        }
        WAITV(0);
        BAR();
        STAMP(17);
        f32x16 a0 = {}, a1 = {};
        u32x4 b0[3], b1[3];
        b0[0] = lds16(L_A + lo); b1[0] = lds16(L_A + KB + lo); b0[1] = lds16(L_A + 2 * KB + lo); b1[1] = lds16(L_A + 3 * KB + lo);
#pragma unroll
        for (int i = 18; i < 28; ++i) fr[i] = W1RD(i - 18);
        float mean[2], rs[2];
#pragma unroll
        for (int m = 0; m < 2; ++m) {
            float s1 = 0.f, s2 = 0.f;
#pragma unroll
            for (int i = 0; i < 8; ++i) { const f32x2 v = *(const LAS f32x2*)(uintptr_t)(L_M + i * 512 + (m * 32 + r) * 8); s1 += v[0]; s2 += v[1]; }
            mean[m] = s1 * (1.f / DM); rs[m] = __builtin_amdgcn_rsqf(fmaxf(s2 * (1.f / DM) - mean[m] * mean[m], 0.f) + 1e-5f);
            PIN(mean[m]); PIN(rs[m]);
        }
        rstd2 = mt ? rs[1] : rs[0];
#pragma unroll
        for (int i = 18; i < 28; ++i) PIN(fr[i]);
        BARX();
#pragma unroll
        for (int s = 0; s < 28; ++s) {
            if (s + 2 < 28) { const int n = s + 2; b0[n % 3] = lds16(L_A + (2 * n) * KB + lo); b1[n % 3] = lds16(L_A + (2 * n + 1) * KB + lo); }
            SB();
            a0 = MFMA(fr[s], b0[s % 3], a0); a1 = MFMA(fr[s], b1[s % 3], a1);
            PIN(a0); PIN(a1);
            if (s < 5) W2PAIR(2 + 2 * s + (w >> 2), w & 3, L_RING + (2 * s + (w >> 2)) * 8 * KB);
            SB();
        }
        STAMP(18);
#pragma unroll
        for (int m = 0; m < 2; ++m) {
            const f32x16& a = m ? a1 : a0;
            const float ce = rs[m] * -1.4426950408889634f;
            u32x4 o[2];
#pragma unroll
            for (int g = 0; g < 4; ++g) {
                float y[4];
#pragma unroll
                for (int e = 0; e < 4; ++e) { const float z = fmaf(-mean[m], cs[g][e], a[4 * g + e]); y[e] = z * __builtin_amdgcn_rcpf(1.f + EX2(z * ce)); }
                o[g >> 1][(g & 1) * 2] = pk(y[0], y[1]); o[g >> 1][(g & 1) * 2 + 1] = pk(y[2], y[3]);
            }
            const unsigned so = WS_H1 + ((tt0 + m) * 112 + cb * 16 + w * 2) * 1024;
            st16(rWs, o[0], lo, so); st16(rWs, o[1], lo + 1024, so);
        }
        STAMP(19);
        WAITV(0);
        BAR();
    }
    STAMP(7);
    {
        if (tid == 0) set_flag(ws, WS_F4 + (tb3 * 2 + mh) * 256 + cb * 8, epoch);
        if (w >= 4) {
#pragma unroll
            for (int i = 0; i < 7; ++i) W2PAIR(12 + i, w - 4, L_A + i * 8 * KB);
        }
        unsigned wbA = trp + kq * 2 * KB, wbB = wbA + L_RING, wbC = wbB + 48 * KB;
        PIN(wbA); PIN(wbB); PIN(wbC);
#define RINGRD(i, n) ((i) < 6 ? wfrag<512>(wbB + (i) * 8 * KB + (n) * KB) : wfrag<512>(wbC + ((i) - 6) * 8 * KB + (n) * KB))
#define W2RD(t, n) ((t) < 12 ? RINGRD(((t) + 10) % 12, n) : (t) < 19 ? wfrag<512>(wbA + ((t) - 12) * 8 * KB + (n) * KB) : RINGRD(((t) - 9) % 12, n))
#pragma unroll
        for (int i = 0; i < 18; ++i) fr[i] = W2RD(i >> 1, i & 1);
#pragma unroll
        for (int i = 0; i < 18; ++i) PIN(fr[i]);
        BAR();
        if (w >= 4) {
#pragma unroll
            for (int i = 0; i < 9; ++i) W2PAIR(19 + i, w - 4, L_RING + ((i + 10) % 12) * 8 * KB);
        } else if (w == 0) wait_flags(ws, WS_F4 + (tb3 * 2 + mh) * 256, 7, epoch, lane);
        BARX();
        STAMP(8);
        u32x4 bf[28];
        const unsigned bs = WS_H1 + ((tt0 + mt) * 112 + 28 * kq) * 1024;
#pragma unroll
        for (int i = 0; i < 28; ++i) bf[i] = ld16(rWs, lo + (i & 3) * 1024, bs + (i >> 2) * 4096);
        SB();
        f32x16 a0 = {}, a1 = {};
        u32x4 f0[3], f1[3];
#pragma unroll
        for (int t = 0; t < 28; ++t) {
            if (t + 2 < 28 && t + 2 >= 9) {
                if (t + 2 == 12) BARX();
                f0[(t + 2) % 3] = W2RD(t + 2, 0); f1[(t + 2) % 3] = W2RD(t + 2, 1);
            }
            SB();
            a0 = MFMA(t < 9 ? fr[2 * t] : f0[t % 3], bf[t], a0); a1 = MFMA(t < 9 ? fr[2 * t + 1] : f1[t % 3], bf[t], a1);
            PIN(a0); PIN(a1);
            SB();
            if (t == 0) STAMP(20); if (t == 27) STAMP(21);
            if (t == 18) BARX();
        }
        float o8[8];
        RED_WRITE(a0, a1);
        BAR();
        RED_READ();
        u32x2 y0 = {pk(fmaf(o8[0], rstd2, bflo(hres[0])), fmaf(o8[1], rstd2, bfhi(hres[0]))), pk(fmaf(o8[2], rstd2, bflo(hres[1])), fmaf(o8[3], rstd2, bfhi(hres[1])))};
        u32x2 y1 = {pk(fmaf(o8[4], rstd2, bflo(hres[2])), fmaf(o8[5], rstd2, bfhi(hres[2]))), pk(fmaf(o8[6], rstd2, bflo(hres[3])), fmaf(o8[7], rstd2, bfhi(hres[3])))};
        __builtin_amdgcn_raw_buffer_store_b64(y0, rOut, xo, 0, 0);
        __builtin_amdgcn_raw_buffer_store_b64(y1, rOut, xo + 16, 0, 0);
    }
    STAMP(9);
}

extern "C" void kernel_launch(void* const* d_in, const int* in_sizes, int n_in, void* d_out, int out_size, void* d_ws, size_t ws_size,
                              hipStream_t stream) {
    (void)hipFuncSetAttribute((const void*)block_kernel, hipFuncAttributeMaxDynamicSharedMemorySize, LDS_BYTES);
    block_kernel<<<256, 512, LDS_BYTES, stream>>>(d_in[0], d_in[1], d_in[2], d_in[4], d_in[5], d_out, (char*)d_ws);
}
```

```cpp
#include <hip/hip_runtime.h>
#include <stdint.h>

typedef __attribute__((ext_vector_type(8))) __bf16 bf16x8;
typedef __attribute__((ext_vector_type(2))) __bf16 bf16x2;
typedef __attribute__((ext_vector_type(4))) short s16x4;
typedef __attribute__((ext_vector_type(16))) float f32x16;
typedef __attribute__((ext_vector_type(4))) float f32x4;
typedef __attribute__((ext_vector_type(2))) float f32x2;
typedef __attribute__((ext_vector_type(4))) unsigned u32x4;
typedef __attribute__((ext_vector_type(2))) unsigned u32x2;
typedef unsigned long long u64;
#define LAS __attribute__((address_space(3)))
#define GAS __attribute__((address_space(1)))
#define DI __device__ __forceinline__
typedef __amdgpu_buffer_rsrc_t rsrc_t;

constexpr int SEQ = 2048, DM = 448, NH = 16, DH = 28, FF = 1792;
constexpr unsigned WS_EPOCH = 0;
constexpr unsigned WS_F1 = 4096;
constexpr unsigned WS_FS = WS_F1 + 16 * 256;
constexpr unsigned WS_F2 = WS_FS + 1024;
constexpr unsigned WS_F3 = WS_F2 + 16 * 256;
constexpr unsigned WS_F4 = WS_F3 + 32 * 256;
constexpr unsigned WS_CS0 = 65536;
constexpr unsigned WS_CS1 = WS_CS0 + 21 * 256;
constexpr unsigned WS_STAMP = 32u << 20;
constexpr unsigned WS_Q = 131072;
constexpr unsigned WS_K = WS_Q + 64 * 28 * 1024;
constexpr unsigned WS_V = WS_K + 64 * 28 * 1024;
constexpr unsigned WS_O = WS_V + 64 * 28 * 1024;
constexpr unsigned WS_H = WS_O + 64 * 28 * 1024;
constexpr unsigned WS_ST = WS_H + 64 * 28 * 1024;
constexpr unsigned WS_H1 = WS_ST + 64 * 28 * 256;
constexpr unsigned WS_END = WS_H1 + 64 * 112 * 1024;
constexpr unsigned KB = 1024;
constexpr unsigned L_XR = 0, L_WR = 64 * KB, L_CS = 148 * KB, L_XS = L_CS + 512;
constexpr unsigned L_A = 0, L_RING = 56 * KB, L_M = 152 * KB;
constexpr unsigned L_SCR = L_RING + 6 * 8 * KB;
constexpr unsigned L_BAD = 159 * KB, L_CNT = L_BAD + 4, L_GO = L_BAD + 8;
constexpr unsigned LDS_BYTES = 160 * KB;
constexpr unsigned SPIN_CAP = 1u << 16;
constexpr unsigned OOB = 0x7f000000u;

#define WSZ(x) (x)
#define MFMA(a, b, c) __builtin_amdgcn_mfma_f32_32x32x16_bf16(__builtin_bit_cast(bf16x8, a), __builtin_bit_cast(bf16x8, b), c, 0, 0, 0)
#define BAR() asm volatile("s_waitcnt lgkmcnt(0)\n\ts_barrier" ::: "memory")
#define BARX() asm volatile("s_barrier" ::: "memory")
#define LGK() asm volatile("s_waitcnt lgkmcnt(0)" ::: "memory")
#define SB() __builtin_amdgcn_sched_barrier(0)
#define PIN(x) asm volatile("" : "+v"(x))
#define EX2(x) __builtin_amdgcn_exp2f(x)
#define RLX_AGENT __ATOMIC_RELAXED, __HIP_MEMORY_SCOPE_AGENT

#define STAMP(i) do {} while (0)

template <int N> DI void waitv() { asm volatile("s_waitcnt vmcnt(%0)" ::"n"(N) : "memory"); }
DI void dma(rsrc_t rs, unsigned voff, unsigned soff, unsigned lds) {
    asm volatile("s_mov_b32 m0, %0\n\ts_nop 0\n\tbuffer_load_dwordx4 %1, %2, %3 offen lds" ::"s"(lds), "v"(voff), "s"(rs), "s"(soff) : "memory");
}
DI void dma_sc1(rsrc_t rs, unsigned voff, unsigned soff, unsigned lds) {
    asm volatile("s_mov_b32 m0, %0\n\ts_nop 0\n\tbuffer_load_dwordx4 %1, %2, %3 offen sc1 lds" ::"s"(lds), "v"(voff), "s"(rs), "s"(soff) : "memory");
}
DI u32x4 ld16(rsrc_t rs, unsigned voff, unsigned soff) { return __builtin_amdgcn_raw_buffer_load_b128(rs, voff, soff, 16); }
DI void st16(rsrc_t rs, u32x4 v, unsigned voff, unsigned soff) { __builtin_amdgcn_raw_buffer_store_b128(v, rs, voff + soff, 0, 16); }
DI void st8(rsrc_t rs, u32x2 v, unsigned voff, unsigned soff) { __builtin_amdgcn_raw_buffer_store_b64(v, rs, voff, soff, 16); }
DI u32x4 lds16(unsigned a) { return *(const LAS u32x4*)(uintptr_t)a; }
DI f32x4 lds16f(unsigned a) { return *(const LAS f32x4*)(uintptr_t)a; }
DI u32x2 ldstr(unsigned a) { return __builtin_bit_cast(u32x2, __builtin_amdgcn_ds_read_tr16_b64_v4i16((LAS s16x4*)(uintptr_t)a)); }
template <int D2> DI u32x4 wfrag(unsigned a) { u32x2 lo = ldstr(a), hi = ldstr(a + D2); return u32x4{lo[0], lo[1], hi[0], hi[1]}; }
DI unsigned pk(float lo, float hi) { return __builtin_bit_cast(unsigned, __builtin_convertvector(f32x2{lo, hi}, bf16x2)); }
DI float bflo(unsigned u) { return __uint_as_float(u << 16); }
DI float bfhi(unsigned u) { return __uint_as_float(u & 0xffff0000u); }
DI float sum32(float v) {
    auto r = __builtin_amdgcn_permlane32_swap(__float_as_uint(v), __float_as_uint(v), false, false);
    return __uint_as_float(r[0]) + __uint_as_float(r[1]);
}
DI float max32(float v) {
    auto r = __builtin_amdgcn_permlane32_swap(__float_as_uint(v), __float_as_uint(v), false, false);
    return fmaxf(__uint_as_float(r[0]), __uint_as_float(r[1]));
}
DI int fresh_lane() { int l; asm volatile("v_mbcnt_lo_u32_b32 %0, -1, 0\n\tv_mbcnt_hi_u32_b32 %0, -1, %0" : "=v"(l)); return l; }
DI float add1(float a, float b) { float d; asm("v_add_f32 %0, %1, %2" : "=v"(d) : "v"(a), "v"(b)); return d; }
DI float mul1(float a, float b) { float d; asm("v_mul_f32 %0, %1, %2" : "=v"(d) : "v"(a), "v"(b)); return d; }
DI float fma1(float a, float b, float c) { float d; asm("v_fma_f32 %0, %1, %2, %3" : "=v"(d) : "v"(a), "v"(b), "v"(c)); return d; }
DI u32x2 silu4(float a0, float a1, float a2, float a3, f32x4 c, float nm, float ce) {
    unsigned o0, o1; float z0, z1, z2, z3, t0, t1, t2, t3;
    asm("v_fma_f32 %2, %14, %10, %16\n\tv_fma_f32 %3, %14, %11, %17\n\tv_fma_f32 %4, %14, %12, %18\n\tv_fma_f32 %5, %14, %13, %19\n\t"
        "v_mul_f32 %6, %15, %2\n\tv_mul_f32 %7, %15, %3\n\tv_mul_f32 %8, %15, %4\n\tv_mul_f32 %9, %15, %5\n\t"
        "v_exp_f32 %6, %6\n\tv_exp_f32 %7, %7\n\tv_exp_f32 %8, %8\n\tv_exp_f32 %9, %9\n\t"
        "v_add_f32 %6, 1.0, %6\n\tv_add_f32 %7, 1.0, %7\n\tv_add_f32 %8, 1.0, %8\n\tv_add_f32 %9, 1.0, %9\n\t"
        "v_rcp_f32 %6, %6\n\tv_rcp_f32 %7, %7\n\tv_rcp_f32 %8, %8\n\tv_rcp_f32 %9, %9\n\t"
        "v_mul_f32 %2, %2, %6\n\tv_mul_f32 %3, %3, %7\n\tv_mul_f32 %4, %4, %8\n\tv_mul_f32 %5, %5, %9\n\t"
        "v_cvt_pk_bf16_f32 %0, %2, %3\n\tv_cvt_pk_bf16_f32 %1, %4, %5"
        : "=v"(o0), "=v"(o1), "=&v"(z0), "=&v"(z1), "=&v"(z2), "=&v"(z3), "=&v"(t0), "=&v"(t1), "=&v"(t2), "=&v"(t3)
        : "v"(c[0]), "v"(c[1]), "v"(c[2]), "v"(c[3]), "v"(nm), "v"(ce), "v"(a0), "v"(a1), "v"(a2), "v"(a3));
    return u32x2{o0, o1};
}
DI float dot2(unsigned a, unsigned b, float c) { return __builtin_amdgcn_fdot2_f32_bf16(__builtin_bit_cast(bf16x2, a), __builtin_bit_cast(bf16x2, b), c, false); }

DI void wait_flags(char* ws, unsigned off, u64 epoch) {
    const GAS u64* p = (const GAS u64*)(ws + off);
    for (unsigned sp = 0; sp < SPIN_CAP; ++sp) {
        const u64 v = __hip_atomic_load(p, RLX_AGENT);
        if (__all(v == epoch)) break;
    }
    asm volatile("" ::: "memory");
}
DI void wait_flags2(char* ws, unsigned off0, unsigned off1, u64 epoch) {
    const GAS u64* p0 = (const GAS u64*)(ws + off0); const GAS u64* p1 = (const GAS u64*)(ws + off1);
    for (unsigned sp = 0; sp < SPIN_CAP; ++sp) {
        const u64 v0 = __hip_atomic_load(p0, RLX_AGENT), v1 = __hip_atomic_load(p1, RLX_AGENT);
        if (__all(v0 == epoch && v1 == epoch)) break;
    }
    asm volatile("" ::: "memory");
}
DI void set_flag(char* ws, unsigned off, u64 epoch) { __hip_atomic_store((GAS u64*)(ws + off), epoch, RLX_AGENT); }

__global__ void __launch_bounds__(512, 2)
block_kernel(const void* __restrict__ gx, const void* __restrict__ gwqkv, const void* __restrict__ gwo, const void* __restrict__ gw1,
             const void* __restrict__ gw2, void* __restrict__ gout, char* __restrict__ ws) {
    extern __shared__ __attribute__((aligned(16))) char smem[];
    const int tid = threadIdx.x;
    const int w = __builtin_amdgcn_readfirstlane(tid >> 6);
    const int b = blockIdx.x, xcd = b & 7, bi = b >> 3;
    const rsrc_t rX = __builtin_amdgcn_make_buffer_rsrc((void*)gx, 0, SEQ * DM * 2, 0x00020000);
    const rsrc_t rWqkv = __builtin_amdgcn_make_buffer_rsrc((void*)gwqkv, 0, DM * 3 * DM * 2, 0x00020000);
    const rsrc_t rW1 = __builtin_amdgcn_make_buffer_rsrc((void*)gw1, 0, WSZ(DM * FF * 2), 0x00020000);
    const rsrc_t rW2 = __builtin_amdgcn_make_buffer_rsrc((void*)gw2, 0, WSZ(FF * DM * 2), 0x00020000);
    const rsrc_t rOut = __builtin_amdgcn_make_buffer_rsrc(gout, 0, SEQ * DM * 2, 0x00020000);
    const rsrc_t rWs = __builtin_amdgcn_make_buffer_rsrc(ws, 0, WS_END, 0x00020000);
    const int tb1 = 4 * (xcd >> 1) + bi / 7, c1 = 7 * (xcd & 1) + bi % 7;
    const int h2 = 2 * xcd + (bi & 1), qb = bi >> 1;
    const int tb3 = 2 * xcd + (bi >> 4), j3 = bi & 15, mh = j3 & 1, cb = j3 >> 1;
    const bool act3 = j3 < 14;
    STAMP(31); STAMP(0);
    const u64 epoch = *(const __attribute__((address_space(4))) u64*)(ws + WS_EPOCH) + 1;
    {
    const int lane = tid & 63, r = lane & 31, hh = lane >> 5;
    const unsigned trn = (unsigned)((8 * hh + ((lane & 15) >> 2)) * 64 + ((lane >> 4) & 1) * 32 + (lane & 3) * 8);
    const unsigned trp = (unsigned)((4 * hh + ((lane & 15) >> 2)) * 64 + ((lane >> 4) & 1) * 32 + (lane & 3) * 8);
    const unsigned v1 = (lane >> 2) * 3584 + (lane & 3) * 16;
    const unsigned v2 = (lane >> 2) * 896 + (lane & 3) * 16;
    const unsigned lo = lane * 16;
    if (tid == 0) *(LAS u32x4*)(uintptr_t)L_BAD = u32x4{0u, 0u, 0u, 0u};

#define EARLY_W(j) do { if (act3) { \
            const rsrc_t rWo = __builtin_amdgcn_make_buffer_rsrc((void*)gwo, 0, WSZ(DM * DM * 2), 0x00020000); \
            _Pragma("unroll") for (int i = 0; i < 7; ++i) { const int id = 4 * i + (j); dma(rWo, v2, (unsigned)(id * 16 * 896 + cb * 128), L_A + id * 2 * KB); dma(rWo, v2, (unsigned)(id * 16 * 896 + cb * 128 + 64), L_A + id * 2 * KB + KB); } \
            _Pragma("unroll") for (int i = 0; i < 6; ++i) { dma(rW1, v1, (unsigned)(i * 16 * 3584 + cb * 512 + (j) * 128), L_RING + (i * 4 + (j)) * 2 * KB); dma(rW1, v1, (unsigned)(i * 16 * 3584 + cb * 512 + (j) * 128 + 64), L_RING + (i * 4 + (j)) * 2 * KB + KB); } } \
        } while (0)
#define TOUCH_W(j) do { if ((j) == 0) dma(rW1, lane * 128, (unsigned)b * 6272, L_M + 4096); if ((j) == 1) dma(rW2, lane * 128, (unsigned)b * 6272, L_M + 4096); \
        if ((j) >= 2) dma(__builtin_amdgcn_make_buffer_rsrc((void*)gwo, 0, DM * DM * 2, 0x00020000), lane < 13 ? lane * 128 : OOB, (j) == 2 ? (unsigned)b * 1568 : OOB, L_M + 4096); } while (0)
    if (w == 0) dma(rX, lane < 56 ? lane * 128 : OOB, (unsigned)b * 7168, L_M + 4096);
    if (w == 1) dma(rWqkv, lane < 37 ? lane * 128 : OOB, (unsigned)b * 4704, L_M + 4096);
    if (bi >= 28) {
        const int id = (bi - 28) * 8 + xcd;
        const int sub = lane & 7, rw = lane >> 3;
#pragma unroll 1
        for (int job = 21 + id; job < 49; job += 32) {
            const bool q = job < 21;
            const rsrc_t rs = __builtin_amdgcn_make_buffer_rsrc((void*)(q ? gwqkv : gw1), 0, q ? DM * 3 * DM * 2 : DM * FF * 2, 0x00020000);
            const unsigned stride = q ? 2688 : 3584;
            u32x4 wa[7];
#pragma unroll
            for (int i = 0; i < 7; ++i) wa[i] = __builtin_amdgcn_raw_buffer_load_b128(rs, (unsigned)(w * 56 + i * 8 + rw) * stride + sub * 16, (unsigned)(q ? job : job - 21) * 128, 0);
            float c[8] = {0.f, 0.f, 0.f, 0.f, 0.f, 0.f, 0.f, 0.f};
#pragma unroll
            for (int i = 0; i < 7; ++i)
#pragma unroll
                for (int e = 0; e < 4; ++e) { c[2 * e] += bflo(wa[i][e]); c[2 * e + 1] += bfhi(wa[i][e]); }
            *(LAS f32x4*)(uintptr_t)(tid * 32) = f32x4{c[0], c[1], c[2], c[3]}; *(LAS f32x4*)(uintptr_t)(tid * 32 + 16) = f32x4{c[4], c[5], c[6], c[7]};
            BAR();
            { float t = 0.f;
#pragma unroll
              for (int i = 0; i < 8; ++i) t += *(const LAS float*)(uintptr_t)((w * 8 + i) * 256 + lane * 4);
              *(LAS float*)(uintptr_t)(16384 + tid * 4) = t; }
            BAR();
            if (w == 0) {
                float t = 0.f;
#pragma unroll
                for (int i = 0; i < 8; ++i) t += *(const LAS float*)(uintptr_t)(16384 + i * 256 + lane * 4);
                __builtin_amdgcn_raw_buffer_store_b32(__float_as_uint(t), rWs, lane * 4, WS_CS0 + (unsigned)job * 256, 16);
                waitv<0>();
                if (lane == 0) set_flag(ws, WS_FS + job * 8, epoch);
            }
        }
        BAR();
        if (w >= 4) TOUCH_W(w - 4);
    } else {
#define ROTV 0
      const int rot = ROTV == 0 ? 0 : ROTV == 1 ? (c1 + 2 * tb1) % 7 : ((c1 + tb1) & 1);
      if (w < 4) {
        const int rin = lane >> 3, sl = lane & 7;
        const unsigned ve = rin * 896 + ((sl ^ (rin >> 1)) * 16), vo = rin * 896 + ((sl ^ (4 + (rin >> 1))) * 16);
        const unsigned xs = (unsigned)(tb1 * 128 + w * 32) * 896, xl = L_XR + w * 16 * KB;
#define ROT(c) ((c) + rot >= 7 ? (c) + rot - 7 : (c) + rot)
#define XDMA(c) do { const unsigned o_ = xs + ROT(c) * 128; _Pragma("unroll") for (int g = 0; g < 4; ++g) dma(rX, (g & 1) ? vo : ve, o_ + g * 8 * 896, xl + ((c) & 3) * 4 * KB + g * KB); } while (0)
        XDMA(0); XDMA(1);
        const unsigned xb = xl + (r >> 3) * KB + (r & 7) * 128 + ((hh ^ ((r >> 1) & 7)) * 16);
        u32x4 xf[8];
#define XRD(c) do { _Pragma("unroll") for (int s = 0; s < 4; ++s) xf[4 * ((c) & 1) + s] = lds16((xb ^ (s * 32)) + ((c) & 3) * 4 * KB); } while (0)
        f32x16 ac[3] = {{}, {}, {}};
        unsigned wbA = L_WR + trn, wbB = L_WR + trn + 63 * KB;
        PIN(wbA); PIN(wbB);
        u32x4 fw[3][3];
#define WRD(ks) do { _Pragma("unroll") for (int t = 0; t < 3; ++t) fw[(ks) % 3][t] = (ks) < 21 ? wfrag<256>(wbA + ((ks) * 3 + t) * KB) : wfrag<256>(wbB + (((ks) - 21) * 3 + t) * KB); } while (0)
        float s1 = 0.f, s2 = 0.f;
#define XST(c) do { _Pragma("unroll") for (int q = 0; q < 4; ++q) _Pragma("unroll") for (int e = 0; e < 4; ++e) { s1 = dot2(xf[4 * ((c) & 1) + q][e], 0x3F803F80u, s1); s2 = dot2(xf[4 * ((c) & 1) + q][e], xf[4 * ((c) & 1) + q][e], s2); } PIN(s1); PIN(s2); SB(); } while (0)
        waitv<4>(); XRD(0); XDMA(2); XST(0);
        BARX(); WRD(0); WRD(1); SB();
#pragma unroll
        for (int ks = 0; ks < 28; ++ks) {
            ac[0] = MFMA(fw[ks % 3][0], xf[ks & 7], ac[0]); PIN(ac[0]);
            SB();
            if (ks + 2 < 28) {
                if ((ks + 2) % 4 == 0) {
                    const int c = (ks + 2) / 4;
                    if (c < 6) waitv<4>(); else waitv<0>();
                    XRD(c);
                    if (c + 2 < 7) XDMA(c + 2);
                    BARX();
                }
                WRD(ks + 2);
                if ((ks + 2) % 4 == 1 && ks > 2) XST((ks + 2) / 4);
            }
            SB();
            ac[1] = MFMA(fw[ks % 3][1], xf[ks & 7], ac[1]); PIN(ac[1]); ac[2] = MFMA(fw[ks % 3][2], xf[ks & 7], ac[2]); PIN(ac[2]);
            SB();
        }
        STAMP(12);
        s1 = sum32(s1); s2 = sum32(s2);
        const float mean = s1 * (1.f / DM), rstd = __builtin_amdgcn_rsqf(fmaxf(s2 * (1.f / DM) - mean * mean, 0.f) + 1e-5f);
        BAR();
        STAMP(13);
        f32x4 csr[3][4];
#pragma unroll
        for (int t = 0; t < 3; ++t)
#pragma unroll
            for (int g = 0; g < 4; ++g) csr[t][g] = lds16f(L_CS + t * 128 + (8 * g + 4 * hh) * 4);
        const unsigned kt = (unsigned)(tb1 * 4 + w);
        u32x2 pkd[3][4];
#pragma unroll
        for (int t = 0; t < 3; ++t) {
            const float sc = 3 * c1 + t < 14 ? rstd * (0.18898223650461362f * 1.4426950408889634f) : rstd, ms = -mean * sc;
#pragma unroll
            for (int g = 0; g < 4; ++g) {
                float y[4];
#pragma unroll
                for (int e = 0; e < 4; ++e) y[e] = fmaf(ac[t][4 * g + e], sc, ms * csr[t][g][e]);
                pkd[t][g] = u32x2{pk(y[0], y[1]), pk(y[2], y[3])};
            }
            if (3 * c1 + t >= 28) {
#pragma unroll
                for (int g = 0; g < 4; ++g) *(LAS u32x2*)(uintptr_t)(L_SCR + (w * 3 + t) * 2 * KB + r * 64 + (2 * g + hh) * 8) = pkd[t][g];
            } else {
                const int nt = 3 * c1 + t;
                const unsigned base = WS_Q + kt * 28672 + (nt < 14 ? (unsigned)nt * 2048 : WS_K - WS_Q + (unsigned)(nt - 14) * 2048);
#pragma unroll
                for (int g = 0; g < 4; g += 2) {
                    const auto x0 = __builtin_amdgcn_permlane32_swap(pkd[t][g][0], pkd[t][g + 1][0], false, false), x1 = __builtin_amdgcn_permlane32_swap(pkd[t][g][1], pkd[t][g + 1][1], false, false);
                    st16(rWs, u32x4{x0[0], x1[0], x0[1], x1[1]}, lo + g * 512, base);
                }
            }
        }
        if (c1 >= 9) {
            LGK();
#pragma unroll
            for (int t = 0; t < 3; ++t) {
                if (3 * c1 + t >= 28) {
                    const u32x4 f0 = wfrag<512>(L_SCR + (w * 3 + t) * 2 * KB + trp), f1 = wfrag<512>(L_SCR + (w * 3 + t) * 2 * KB + trp + 1024);
                    const unsigned off = (unsigned)(hh * 7168 + ((3 * c1 + t - 28) * 32 + r) * 16);
                    st16(rWs, f0, off, WS_V + kt * 28672); st16(rWs, f1, off + 14336, WS_V + kt * 28672);
                }
            }
        }
        STAMP(14);
        waitv<0>();
        if (lane == 0 && __hip_atomic_fetch_add((LAS unsigned*)(uintptr_t)L_CNT, 1u, __ATOMIC_RELAXED, __HIP_MEMORY_SCOPE_WORKGROUP) == 3u) set_flag(ws, WS_F1 + c1 * 256 + tb1 * 8, epoch);
      } else {
        const int j = w - 4;
        const unsigned vw = (lane >> 2) * 2688 + (lane & 3) * 16;
        const unsigned so = (unsigned)(j * 16 * 2688 + c1 * 192), ld = L_WR + j * 3 * KB;
#define WDMA(st) do { const unsigned o_ = so + ROT(st) * 64 * 2688; dma(rWqkv, vw, o_, ld + (st) * 12 * KB); dma(rWqkv, vw, o_ + 64, ld + (st) * 12 * KB + KB); dma(rWqkv, vw, o_ + 128, ld + (st) * 12 * KB + 2 * KB); } while (0)
        WDMA(0); WDMA(1);
        const u32x4 ones = {0x3F803F80u, 0x3F803F80u, 0x3F803F80u, 0x3F803F80u};
        f32x16 cacc = {};
        unsigned wb = L_WR + trn + (j < 3 ? j : 0) * KB;
#pragma unroll
        for (int st = 0; st < 7; ++st) {
            if (st < 5) { waitv<3>(); WDMA(st + 2); } else if (st == 5) waitv<4>(); else waitv<1>();
            if (st == 4) TOUCH_W(j);
            BARX();
            if (j < 3) {
                u32x4 f[4];
#pragma unroll
                for (int q = 0; q < 4; ++q) f[q] = wfrag<256>(wb + q * 3 * KB);
#pragma unroll
                for (int q = 0; q < 4; ++q) cacc = MFMA(ones, f[q], cacc);
                wb += 12 * KB; PIN(wb); PIN(cacc);
            }
            if (st == 0) STAMP(10); if (st == 3) STAMP(11);
        }
        if (j < 3 && hh == 0) *(LAS float*)(uintptr_t)(L_CS + j * 128 + r * 4) = cacc[0];
        BAR();
        while (*(const volatile LAS unsigned*)(uintptr_t)L_CNT < 4u) __builtin_amdgcn_s_sleep(1);
      }
    }
    STAMP(1); STAMP(25);

    {
        const int qh = w & 1, kq = w >> 1;
        __builtin_amdgcn_s_setprio(3);
        if (w == 0) {
            const int c0 = (28 * h2) & ~7;
            const int col = lane < 32 ? 448 + c0 + 31 * ((lane >> 4) & 1) : 896 + 28 * h2 + 27 * ((lane >> 4) & 1);
            wait_flags2(ws, WS_F1 + (unsigned)(col / 96) * 256 + (lane & 15) * 8, WS_F1 + (unsigned)((c0 + 31 * (lane & 1)) / 96) * 256 + qb * 8, epoch);
            *(volatile LAS unsigned*)(uintptr_t)L_GO = 1u;
        } else {
            if (w >= 4) EARLY_W(w - 4);
            while (*(const volatile LAS unsigned*)(uintptr_t)L_GO == 0u) __builtin_amdgcn_s_sleep(1);
            asm volatile("" ::: "memory");
        }
        STAMP(2); STAMP(26);
        const unsigned ch = (unsigned)((28 * h2) >> 3) * 512;
        const unsigned qso = WS_Q + (unsigned)(qb * 4 + qh * 2) * 28672 + ch;
        const rsrc_t rK = __builtin_amdgcn_make_buffer_rsrc(ws + WS_K + (unsigned)kq * 16 * 28672 + ch, 0, 15 * 28672 + 2048, 0x00020000);
        const rsrc_t rV = __builtin_amdgcn_make_buffer_rsrc(ws + WS_V + (unsigned)kq * 16 * 28672 + (unsigned)h2 * 448, 0, 16 * 28672 - h2 * 448, 0x00020000);
        const unsigned vo0 = (unsigned)(hh * 7168 + r * 16), vo1 = vo0 + 14336, vpad = r == DH ? 0x3F803F80u : 0u;
        u32x4 qa0 = ld16(rWs, lo, qso), qa1 = ld16(rWs, lo + 1024, qso), qb0 = ld16(rWs, lo, qso + 28672), qb1 = ld16(rWs, lo + 1024, qso + 28672);
        const u32x4 rk0 = ld16(rWs, lo, WS_K + ch), rk1 = ld16(rWs, lo + 1024, WS_K + ch);
#pragma unroll 1
        for (int pass = 0; pass < 2; ++pass) {
            u32x4 kf[4][2], vf[4][2], pa[2], pb[2];
#pragma unroll
            for (int i = 0; i < 4; ++i) {
                kf[i][0] = ld16(rK, lo, i * 28672); kf[i][1] = ld16(rK, lo + 1024, i * 28672);
                vf[i][0] = vf[i][1] = u32x4{vpad, vpad, vpad, vpad};
            }
            if (r < DH) {
#pragma unroll
                for (int i = 0; i < 3; ++i) { vf[i][0] = ld16(rV, vo0, i * 28672); vf[i][1] = ld16(rV, vo1, i * 28672); }
            }
            SB();
            f32x16 oa = {}, ob = {}, sb = {};
            pb[0] = pb[1] = u32x4{0u, 0u, 0u, 0u};
            PIN(oa); PIN(ob); PIN(sb); PIN(vf[3][0]); PIN(vf[3][1]); PIN(pb[0]); PIN(pb[1]);
            SB();
            if (h2 & 1) { if (!hh) { qa0[0] = 0u; qa0[1] = 0u; qb0[0] = 0u; qb0[1] = 0u; } }
            else if (hh) { qa1[2] = 0u; qa1[3] = 0u; qb1[2] = 0u; qb1[3] = 0u; }
            float ma, mb;
            if (pass == 0) {
                f32x16 z = {};
                f32x16 ta = MFMA(rk0, qa0, z), tb = MFMA(rk0, qb0, z); ta = MFMA(rk1, qa1, ta); tb = MFMA(rk1, qb1, tb);
                ma = ta[0]; mb = tb[0];
#pragma unroll
                for (int i = 1; i < 16; ++i) { ma = fmaxf(ma, ta[i]); mb = fmaxf(mb, tb[i]); }
                ma = max32(ma); mb = max32(mb);
            } else {
                f32x16 z = {};
                ma = -INFINITY; mb = -INFINITY;
#pragma unroll 1
                for (int t = 0; t < 16; ++t) {
                    const u32x4 k0 = ld16(rK, lo, t * 28672), k1 = ld16(rK, lo + 1024, t * 28672);
                    f32x16 ta = MFMA(k0, qa0, z), tb = MFMA(k0, qb0, z); ta = MFMA(k1, qa1, ta); tb = MFMA(k1, qb1, tb);
#pragma unroll
                    for (int i = 0; i < 16; ++i) { ma = fmaxf(ma, ta[i]); mb = fmaxf(mb, tb[i]); }
                }
                ma = max32(ma); mb = max32(mb);
                *(LAS float*)(uintptr_t)(L_M + 2048 + ((kq * 2 + qh) * 2 + hh) * 128 + r * 4) = hh ? mb : ma;
                BAR();
#pragma unroll
                for (int i = 0; i < 4; ++i) { ma = fmaxf(ma, *(const LAS float*)(uintptr_t)(L_M + 2048 + ((i * 2 + qh) * 2) * 128 + r * 4)); mb = fmaxf(mb, *(const LAS float*)(uintptr_t)(L_M + 2048 + ((i * 2 + qh) * 2 + 1) * 128 + r * 4)); }
                ma += fabsf(ma) * 9.5367431640625e-07f; mb += fabsf(mb) * 9.5367431640625e-07f;
            }
            f32x16 ca, cbv;
#pragma unroll
            for (int i = 0; i < 16; ++i) { ca[i] = -ma; cbv[i] = -mb; }
            PIN(ca); PIN(cbv);
            f32x16 sa = MFMA(kf[0][0], qa0, ca); sa = MFMA(kf[0][1], qa1, sa);
            PIN(sa); STAMP(15);
            SB();
#define GRP(MF, S, i, T, j, P) do { MF; SB(); \
            S[4 * (i)] = EX2(S[4 * (i)]); S[4 * (i) + 1] = EX2(S[4 * (i) + 1]); S[4 * (i) + 2] = EX2(S[4 * (i) + 2]); S[4 * (i) + 3] = EX2(S[4 * (i) + 3]); SB(); \
            P[(j) >> 1][((j) & 1) * 2] = pk(T[4 * (j)], T[4 * (j) + 1]); P[(j) >> 1][((j) & 1) * 2 + 1] = pk(T[4 * (j) + 2], T[4 * (j) + 3]); PIN(P[(j) >> 1]); PIN(S); SB(); } while (0)
#pragma unroll 1
            for (int tq = 0; tq < 4; ++tq) {
                const unsigned sk = (unsigned)tq * 4 * 28672;
                if (tq == 0) __builtin_amdgcn_s_setprio(3); else if (tq == 1) __builtin_amdgcn_s_setprio(2); else if (tq == 2) __builtin_amdgcn_s_setprio(1); else __builtin_amdgcn_s_setprio(0);
#pragma unroll
                for (int u = 0; u < 4; ++u) {
                    GRP(ob = MFMA(vf[(u + 3) & 3][0], pb[0], ob), sa, 0, sb, 3, pb);
                    GRP(sb = MFMA(kf[u][0], qb0, cbv), sa, 1, sa, 0, pa);
                    GRP(sb = MFMA(kf[u][1], qb1, sb), sa, 2, sa, 1, pa);
                    GRP(ob = MFMA(vf[(u + 3) & 3][1], pb[1], ob), sa, 3, sa, 2, pa);
                    kf[u][0] = ld16(rK, lo, sk + (u + 4) * 28672); kf[u][1] = ld16(rK, lo + 1024, sk + (u + 4) * 28672);
                    if (r < DH) { vf[(u + 3) & 3][0] = ld16(rV, vo0, sk + (u + 3) * 28672); vf[(u + 3) & 3][1] = ld16(rV, vo1, sk + (u + 3) * 28672); }
                    SB();
                    GRP(oa = MFMA(vf[u][0], pa[0], oa), sb, 0, sa, 3, pa);
                    GRP(sa = MFMA(kf[(u + 1) & 3][0], qa0, ca), sb, 1, sb, 0, pb);
                    GRP(sa = MFMA(kf[(u + 1) & 3][1], qa1, sa), sb, 2, sb, 1, pb);
                    GRP(oa = MFMA(vf[u][1], pa[1], oa), sb, 3, sb, 2, pb);
                }
            }
            pb[1][2] = pk(sb[12], sb[13]); pb[1][3] = pk(sb[14], sb[15]);
            ob = MFMA(vf[3][0], pb[0], ob); ob = MFMA(vf[3][1], pb[1], ob);
            waitv<0>();
            PIN(ob); STAMP(16);
            const int lane = fresh_lane(), r = lane & 31, hh = lane >> 5;
            const unsigned lo = lane * 16;
            if (hh) { *(LAS float*)(uintptr_t)(L_M + ((kq * 2 + qh) * 2) * 128 + r * 4) = oa[12]; *(LAS float*)(uintptr_t)(L_M + ((kq * 2 + qh) * 2 + 1) * 128 + r * 4) = ob[12]; }
#pragma unroll
            for (int p = 0; p < 4; ++p) {
                if (p != kq) {
                    const unsigned dst = L_SCR + (unsigned)((qh * 4 + p) * 3 + (kq > p ? kq - 1 : kq)) * 2048 + lo;
                    const f32x16& o = (p >> 1) ? ob : oa;
                    *(LAS f32x4*)(uintptr_t)dst = f32x4{o[8 * (p & 1)], o[8 * (p & 1) + 1], o[8 * (p & 1) + 2], o[8 * (p & 1) + 3]};
                    *(LAS f32x4*)(uintptr_t)(dst + 1024) = f32x4{o[8 * (p & 1) + 4], o[8 * (p & 1) + 5], o[8 * (p & 1) + 6], o[8 * (p & 1) + 7]};
                }
            }
            if (pass == 0) {
                float t = 0.f;
#pragma unroll
                for (int i = 0; i < 13; ++i) t += fabsf(oa[i]) + fabsf(ob[i]);
                float t2 = 0.f;
#pragma unroll
                for (int i = 13; i < 16; ++i) t2 += fabsf(oa[i]) + fabsf(ob[i]);
                if (!hh) t += t2;
                if (__any(!(t < 1.0e36f))) *(LAS unsigned*)(uintptr_t)L_BAD = 1;
            }
            float o8[8];
#pragma unroll
            for (int i = 0; i < 8; ++i) { const float lo8 = (kq & 2) ? ob[i] : oa[i], hi8 = (kq & 2) ? ob[8 + i] : oa[8 + i]; o8[i] = (kq & 1) ? hi8 : lo8; }
            BAR();
            STAMP(22);
            const unsigned badw = *(const volatile LAS unsigned*)(uintptr_t)L_BAD;
            float l = 0.f;
#pragma unroll
            for (int i = 0; i < 4; ++i) l += *(const LAS float*)(uintptr_t)(L_M + ((i * 2 + qh) * 2 + (kq >> 1)) * 128 + r * 4);
#pragma unroll
            for (int i = 0; i < 3; ++i) {
                const unsigned src = L_SCR + (unsigned)((qh * 4 + kq) * 3 + i) * 2048 + lo;
                const f32x4 a = lds16f(src), c = lds16f(src + 1024);
#pragma unroll
                for (int e = 0; e < 4; ++e) { o8[e] += a[e]; o8[4 + e] += c[e]; }
            }
            if (pass == 0 && __builtin_amdgcn_readfirstlane(badw)) continue;
            const float il = __builtin_amdgcn_rcpf(l);
            const unsigned tt = (unsigned)(qb * 4 + qh * 2 + (kq >> 1));
#pragma unroll
            for (int i = 0; i < 2; ++i) {
                const int m = 4 * (kq & 1) + 2 * i + hh, g = 7 * h2 + m;
                st8(rWs, u32x2{pk(o8[4 * i] * il, o8[4 * i + 1] * il), pk(o8[4 * i + 2] * il, o8[4 * i + 3] * il)},
                    m < 7 ? (unsigned)(((g >> 2) * 64 + (g & 1) * 32 + r) * 16 + ((g >> 1) & 1) * 8) : OOB, WS_O + tt * 28 * 1024);
            }
            break;
        }
    }
    }
    const int lane = fresh_lane(), r = lane & 31, hh = lane >> 5;
    const unsigned lo = lane * 16;
    const unsigned trp = (unsigned)((4 * hh + ((lane & 15) >> 2)) * 64 + ((lane >> 4) & 1) * 32 + (lane & 3) * 8);
    const unsigned v1 = (lane >> 2) * 3584 + (lane & 3) * 16, v2 = (lane >> 2) * 896 + (lane & 3) * 16;
    if (!act3) {
        waitv<0>();
        BAR();
        if (tid == 0) set_flag(ws, WS_F2 + qb * 256 + h2 * 8, epoch);
        if (b == 14 * 8 && w == 0) {
#pragma unroll 1
            for (int i = 0; i < 4; ++i) wait_flags(ws, WS_F2 + (unsigned)((i * 4 + (lane >> 4)) * 256 + (lane & 15) * 8), epoch);
            if (lane == 0) set_flag(ws, WS_EPOCH, epoch);
        }
        return;
    }

    const int mt = w & 1, kq = w >> 1;
    const unsigned tt0 = (unsigned)(tb3 * 4 + mh * 2), grp = (unsigned)(tb3 * 2 + mh);
#define W1PAIR(ks, q, ri) do { const unsigned so_ = (unsigned)((ks) * 16 * 3584 + cb * 512 + (q) * 128), ld_ = L_RING + ((ri) * 8 + (q) * 2) * KB; dma(rW1, v1, so_, ld_); dma(rW1, v1, so_ + 64, ld_ + KB); } while (0)
#define R5(t) ((t) + 4 * cb >= 28 ? (t) + 4 * cb - 28 : (t) + 4 * cb)
#define W2PAIR(t, q, row) do { const unsigned so_ = (unsigned)(((q) * 28 + R5(t)) * 16 * 896 + cb * 128), ld_ = (row) + (q) * 2 * KB; dma(rW2, v2, so_, ld_); dma(rW2, v2, so_ + 64, ld_ + KB); } while (0)
    unsigned w1A = L_RING + trp + w * KB, w1B = w1A + 48 * KB;
    PIN(w1A); PIN(w1B);
#define W1RD(i) ((i) < 6 ? wfrag<512>(w1A + (i) * 8 * KB) : wfrag<512>(w1B + ((i) - 6) * 8 * KB))
    u32x4 fr[28];
    const unsigned xo = ((tt0 + mt) * 32 + r) * 896 + (cb * 64 + 16 * kq + 4 * hh) * 2;
#define RED_WRITE(A0, A1) do { _Pragma("unroll") for (int p = 0; p < 4; ++p) { if (p != kq) { \
            const unsigned dst = L_A + (unsigned)((mt * 4 + p) * 3 + (kq > p ? kq - 1 : kq)) * 2048 + lo; const f32x16& o = (p >> 1) ? A1 : A0; \
            *(LAS f32x4*)(uintptr_t)dst = f32x4{o[8 * (p & 1)], o[8 * (p & 1) + 1], o[8 * (p & 1) + 2], o[8 * (p & 1) + 3]}; \
            *(LAS f32x4*)(uintptr_t)(dst + 1024) = f32x4{o[8 * (p & 1) + 4], o[8 * (p & 1) + 5], o[8 * (p & 1) + 6], o[8 * (p & 1) + 7]}; } } \
        _Pragma("unroll") for (int i = 0; i < 8; ++i) { const float lo8 = (kq & 2) ? A1[i] : A0[i], hi8 = (kq & 2) ? A1[8 + i] : A0[8 + i]; o8[i] = (kq & 1) ? hi8 : lo8; } } while (0)
#define RED_READ() do { _Pragma("unroll") for (int i = 0; i < 3; ++i) { const unsigned src = L_A + (unsigned)((mt * 4 + kq) * 3 + i) * 2048 + lo; \
            const f32x4 a = lds16f(src), c = lds16f(src + 1024); _Pragma("unroll") for (int e = 0; e < 4; ++e) { o8[e] += a[e]; o8[4 + e] += c[e]; } } } while (0)
    u32x4 hres;
    {
        u32x4 fa[14];
        const unsigned wb = L_A + trp + kq * 14 * KB;
#pragma unroll
        for (int i = 0; i < 14; ++i) fa[i] = wfrag<512>(wb + i * KB);
#pragma unroll
        for (int i = 0; i < 6; ++i) fr[i] = W1RD(i);
#pragma unroll
        for (int i = 0; i < 14; ++i) PIN(fa[i]);
#pragma unroll
        for (int i = 0; i < 6; ++i) PIN(fr[i]);
        waitv<0>();
        BAR();
        STAMP(3); STAMP(27);
        if (w == 0) {
            if (lane == 0) set_flag(ws, WS_F2 + qb * 256 + h2 * 8, epoch);
            wait_flags(ws, WS_F2 + tb3 * 256 + (lane & 15) * 8, epoch);
        } else if (w >= 4) {
#pragma unroll
            for (int i = 0; i < 6; ++i) W1PAIR(6 + i, w & 3, 6 + i);
        }
        u32x2 xr0 = __builtin_amdgcn_raw_buffer_load_b64(rX, xo, 0, 0), xr1 = __builtin_amdgcn_raw_buffer_load_b64(rX, xo + 16, 0, 0);
        BARX();
        STAMP(4); STAMP(28);
        u32x4 bf[7];
#pragma unroll
        for (int i = 0; i < 7; ++i) bf[i] = ld16(rWs, lo, WS_O + ((tt0 + mt) * 28 + 7 * kq + i) * 1024);
        SB();
        f32x16 a0 = {}, a1 = {};
#pragma unroll
        for (int i = 0; i < 7; ++i) { a0 = MFMA(fa[2 * i], bf[i], a0); a1 = MFMA(fa[2 * i + 1], bf[i], a1); }
        PIN(a0); PIN(a1);
        STAMP(23);
#pragma unroll
        for (int i = 0; i < 3; ++i) W1PAIR(12 + 2 * i + (w >> 2), w & 3, 2 * i + (w >> 2));
        float o8[8];
        RED_WRITE(a0, a1);
        BAR();
        STAMP(24);
        RED_READ();
        hres = u32x4{pk(o8[0] + bflo(xr0[0]), o8[1] + bfhi(xr0[0])), pk(o8[2] + bflo(xr0[1]), o8[3] + bfhi(xr0[1])),
                     pk(o8[4] + bflo(xr1[0]), o8[5] + bfhi(xr1[0])), pk(o8[6] + bflo(xr1[1]), o8[7] + bfhi(xr1[1]))};
        st16(rWs, hres, lo, WS_H + ((tt0 + mt) * 28 + cb * 4 + kq) * 1024);
        float s1 = 0.f, s2 = 0.f;
#pragma unroll
        for (int e = 0; e < 4; ++e) { const float a = bflo(hres[e]), c = bfhi(hres[e]); s1 += a + c; s2 = fmaf(a, a, fmaf(c, c, s2)); }
        s1 = sum32(s1); s2 = sum32(s2);
        st8(rWs, u32x2{__float_as_uint(s1), __float_as_uint(s2)}, hh ? OOB : r * 8, WS_ST + ((tt0 + mt) * 28 + cb * 4 + kq) * 256);
#pragma unroll
        for (int i = 6; i < 12; ++i) fr[i] = W1RD(i);
        waitv<0>();
    }
    STAMP(5);
    float rstd2;
    {
        BAR();
        if (tid == 0) set_flag(ws, WS_F3 + grp * 256 + cb * 8, epoch);
#pragma unroll
        for (int i = 12; i < 18; ++i) fr[i] = W1RD(i - 12);
        if (w >= 4) {
#pragma unroll
            for (int i = 0; i < 6; ++i) W1PAIR(18 + i, w & 3, 6 + i);
        }
#pragma unroll
        for (int i = 6; i < 18; ++i) PIN(fr[i]);
        if (w == 0) wait_flags(ws, (lane & 15) < 7 ? WS_F3 + grp * 256 + (unsigned)(lane & 15) * 8 : WS_FS + (unsigned)(21 + cb * 4 + (lane & 3)) * 8, epoch);
        BARX();
        STAMP(6);
#pragma unroll
        for (int i = 0; i < 7; ++i) { const int p = 8 * i + w; dma_sc1(rWs, lo, WS_H + ((tt0 + (p & 1)) * 28 + (p >> 1)) * 1024, L_A + p * KB); }
#pragma unroll
        for (int i = 0; i < 2; ++i) W1PAIR(24 + 2 * i + (w >> 2), w & 3, 2 * i + (w >> 2));
        f32x4 cs[4];
        {
            u32x2 v[4];
#pragma unroll
            for (int i = 0; i < 4; ++i) v[i] = __builtin_amdgcn_raw_buffer_load_b64(rWs, (i == 3 && hh) ? OOB : (unsigned)((hh * 4 + i) * 4 * 256 + r * 8), WS_ST + ((tt0 + mt) * 28 + kq) * 256, 16);
#pragma unroll
            for (int g = 0; g < 4; ++g) cs[g] = __builtin_bit_cast(f32x4, ld16(rWs, (8 * g + 4 * hh) * 4, WS_CS1 + (unsigned)(cb * 256 + w * 32) * 4));
            const float s1 = (__uint_as_float(v[0][0]) + __uint_as_float(v[1][0])) + (__uint_as_float(v[2][0]) + __uint_as_float(v[3][0]));
            const float s2 = (__uint_as_float(v[0][1]) + __uint_as_float(v[1][1])) + (__uint_as_float(v[2][1]) + __uint_as_float(v[3][1]));
            *(LAS f32x2*)(uintptr_t)(L_M + w * 512 + lane * 8) = f32x2{s1, s2};
        }
#pragma unroll
        for (int g = 0; g < 4; ++g) PIN(cs[g]);
        waitv<0>();
        BAR();
        STAMP(17);
        f32x16 a0 = {}, a1 = {};
        u32x4 b0[3], b1[3];
        b0[0] = lds16(L_A + lo); b1[0] = lds16(L_A + KB + lo); b0[1] = lds16(L_A + 2 * KB + lo); b1[1] = lds16(L_A + 3 * KB + lo);
        W2PAIR(w >> 2, w & 3, L_RING + (4 + (w >> 2)) * 8 * KB);
        SB();
        f32x2 sv[2][8];
        float mean[2], rs[2];
#pragma unroll
        for (int s = 0; s < 28; ++s) {
            if (s + 2 < 28) { const int n = s + 2; b0[n % 3] = lds16(L_A + (2 * n) * KB + lo); b1[n % 3] = lds16(L_A + (2 * n + 1) * KB + lo); }
            if (s < 10) fr[18 + s] = W1RD((s + 6) % 12);
            if (s >= 10 && s < 14) {
#pragma unroll
                for (int i = 0; i < 4; ++i) { const int q = (s - 10) * 4 + i; sv[q >> 3][q & 7] = *(const LAS f32x2*)(uintptr_t)(L_M + (((q & 7) >> 1) * 2 + (q >> 3)) * 512 + ((q & 1) * 32 + r) * 8); }
            }
            SB();
            a0 = MFMA(fr[s], b0[s % 3], a0); a1 = MFMA(fr[s], b1[s % 3], a1);
            PIN(a0); PIN(a1);
            if (s == 12) {
#pragma unroll
                for (int i = 18; i < 28; ++i) PIN(fr[i]);
                BARX();
            }
            if (s >= 12 && s < 17) W2PAIR(2 + 2 * (s - 12) + (w >> 2), w & 3, L_RING + ((6 + 2 * (s - 12) + (w >> 2)) % 12) * 8 * KB);
            if (s == 17 || s == 18) {
                const int m = s - 17;
                float s1 = 0.f, s2 = 0.f;
#pragma unroll
                for (int i = 0; i < 8; ++i) { s1 += sv[m][i][0]; s2 += sv[m][i][1]; }
                mean[m] = s1 * (1.f / DM); rs[m] = __builtin_amdgcn_rsqf(fmaxf(s2 * (1.f / DM) - mean[m] * mean[m], 0.f) + 1e-5f);
                PIN(mean[m]); PIN(rs[m]);
            }
            SB();
        }
        rstd2 = mt ? rs[1] : rs[0];
        asm volatile("s_nop 15" : "+v"(a0), "+v"(a1));
        STAMP(18);
#pragma unroll
        for (int m = 0; m < 2; ++m) {
            const f32x16& a = m ? a1 : a0;
            const float ce = rs[m] * -1.4426950408889634f, nm = -mean[m];
            u32x4 o[2];
#pragma unroll
            for (int g = 0; g < 4; ++g) {
                const u32x2 y = silu4(a[4 * g], a[4 * g + 1], a[4 * g + 2], a[4 * g + 3], cs[g], nm, ce);
                o[g >> 1][(g & 1) * 2] = y[0]; o[g >> 1][(g & 1) * 2 + 1] = y[1];
            }
            const unsigned so = WS_H1 + ((tt0 + m) * 112 + cb * 16 + w * 2) * 1024;
            st16(rWs, o[0], lo, so); st16(rWs, o[1], lo + 1024, so);
        }
        STAMP(19);
        waitv<0>();
    }
    STAMP(7);
    {
        BAR();
        if (tid == 0) set_flag(ws, WS_F4 + grp * 256 + cb * 8, epoch);
        unsigned wbA = trp + kq * 2 * KB, wbB = wbA + L_RING, wbC = wbB + 48 * KB;
        PIN(wbA); PIN(wbB); PIN(wbC);
#define RINGRD(i, n) ((i) < 6 ? wfrag<512>(wbB + (i) * 8 * KB + (n) * KB) : wfrag<512>(wbC + ((i) - 6) * 8 * KB + (n) * KB))
#define W2RD(t, n) ((t) < 12 ? RINGRD(((t) + 4) % 12, n) : (t) < 19 ? wfrag<512>(wbA + ((t) - 12) * 8 * KB + (n) * KB) : RINGRD(((t) - 15) % 12, n))
#pragma unroll
        for (int i = 0; i < 18; ++i) fr[i] = W2RD(i >> 1, i & 1);
        if (w >= 4) {
#pragma unroll
            for (int i = 0; i < 7; ++i) W2PAIR(12 + i, w & 3, L_A + i * 8 * KB);
        }
#pragma unroll
        for (int i = 0; i < 18; ++i) PIN(fr[i]);
        BARX();
        if (w == 0) { wait_flags(ws, WS_F4 + grp * 256 + (unsigned)(lane % 7) * 8, epoch); *(volatile LAS unsigned*)(uintptr_t)L_GO = 2u; }
        else {
            if (w >= 4) {
#pragma unroll
                for (int q = 0; q < 9; ++q) W2PAIR(19 + q, w & 3, L_RING + (q + 4 >= 12 ? q - 8 : q + 4) * 8 * KB);
            }
            while (*(const volatile LAS unsigned*)(uintptr_t)L_GO != 2u) __builtin_amdgcn_s_sleep(1);
            asm volatile("" ::: "memory");
        }
        STAMP(8);
        u32x4 bf[28];
        const unsigned bs = WS_H1 + ((tt0 + mt) * 112 + 28 * kq) * 1024;
#define BLD(g) do { const unsigned o_ = bs + R5(4 * (g)) * 1024; _Pragma("unroll") for (int i = 0; i < 4; ++i) bf[4 * (g) + i] = ld16(rWs, lo + i * 1024, o_); } while (0)
        BLD(0); BLD(1); BLD(2);
        SB();
        f32x16 a0 = {}, a1 = {};
        u32x4 f0[3], f1[3];
#pragma unroll
        for (int t = 0; t < 28; ++t) {
            if (t + 2 < 28 && t + 2 >= 9) {
                if (t + 2 == 12) BARX();
                f0[(t + 2) % 3] = W2RD(t + 2, 0); f1[(t + 2) % 3] = W2RD(t + 2, 1);
            }
            SB();
            a0 = MFMA(t < 9 ? fr[2 * t] : f0[t % 3], bf[t], a0); a1 = MFMA(t < 9 ? fr[2 * t + 1] : f1[t % 3], bf[t], a1);
            PIN(a0); PIN(a1);
            SB();
            if (t % 4 == 0 && t / 4 + 3 < 7) BLD(t / 4 + 3);
            SB();
            if (t == 0) STAMP(20); if (t == 27) STAMP(21);
            if (t == 18) BARX();
        }
        float o8[8];
        RED_WRITE(a0, a1);
        BAR();
        RED_READ();
        u32x2 y0 = {pk(fmaf(o8[0], rstd2, bflo(hres[0])), fmaf(o8[1], rstd2, bfhi(hres[0]))), pk(fmaf(o8[2], rstd2, bflo(hres[1])), fmaf(o8[3], rstd2, bfhi(hres[1])))};
        u32x2 y1 = {pk(fmaf(o8[4], rstd2, bflo(hres[2])), fmaf(o8[5], rstd2, bfhi(hres[2]))), pk(fmaf(o8[6], rstd2, bflo(hres[3])), fmaf(o8[7], rstd2, bfhi(hres[3])))};
        __builtin_amdgcn_raw_buffer_store_b64(y0, rOut, xo, 0, 0);
        __builtin_amdgcn_raw_buffer_store_b64(y1, rOut, xo + 16, 0, 0);
    }
    STAMP(9); STAMP(30);
}

extern "C" void kernel_launch(void* const* d_in, const int* in_sizes, int n_in, void* d_out, int out_size, void* d_ws, size_t ws_size,
                              hipStream_t stream) {
    (void)hipFuncSetAttribute((const void*)block_kernel, hipFuncAttributeMaxDynamicSharedMemorySize, LDS_BYTES);
    block_kernel<<<256, 512, LDS_BYTES, stream>>>(d_in[0], d_in[1], d_in[2], d_in[4], d_in[5], d_out, (char*)d_ws);
}
```

```cpp
#include <hip/hip_runtime.h>
#include <stdint.h>

typedef __attribute__((ext_vector_type(8))) __bf16 bf16x8;
typedef __attribute__((ext_vector_type(2))) __bf16 bf16x2;
typedef __attribute__((ext_vector_type(4))) short s16x4;
typedef __attribute__((ext_vector_type(16))) float f32x16;
typedef __attribute__((ext_vector_type(4))) float f32x4;
typedef __attribute__((ext_vector_type(2))) float f32x2;
typedef __attribute__((ext_vector_type(4))) unsigned u32x4;
typedef __attribute__((ext_vector_type(2))) unsigned u32x2;
typedef unsigned long long u64;
#define LAS __attribute__((address_space(3)))
#define GAS __attribute__((address_space(1)))
#define DI __device__ __forceinline__
typedef __amdgpu_buffer_rsrc_t rsrc_t;

constexpr int SEQ = 2048, DM = 448, NH = 16, DH = 28, FF = 1792;
constexpr unsigned WS_EPOCH = 0;
constexpr unsigned WS_F1 = 4096;
constexpr unsigned WS_FS = WS_F1 + 16 * 256;
constexpr unsigned WS_F2 = WS_FS + 1024;
constexpr unsigned WS_F3 = WS_F2 + 16 * 256;
constexpr unsigned WS_F4 = WS_F3 + 32 * 256;
constexpr unsigned WS_CS0 = 65536;
constexpr unsigned WS_CS1 = WS_CS0 + 21 * 256;
constexpr unsigned WS_STAMP = 32u << 20;
constexpr unsigned WS_Q = 131072;
constexpr unsigned WS_K = WS_Q + 64 * 28 * 1024;
constexpr unsigned WS_V = WS_K + 64 * 28 * 1024;
constexpr unsigned WS_O = WS_V + 64 * 28 * 1024;
constexpr unsigned WS_H = WS_O + 64 * 28 * 1024;
constexpr unsigned WS_ST = WS_H + 64 * 28 * 1024;
constexpr unsigned WS_H1 = WS_ST + 64 * 28 * 256;
constexpr unsigned WS_W1T = WS_H1 + 64 * 112 * 1024;
constexpr unsigned WS_W2T = WS_W1T + 56 * 28 * 1024;
constexpr unsigned WS_WOT = WS_W2T + 14 * 112 * 1024;
constexpr unsigned WS_END = WS_WOT + 14 * 28 * 1024;
constexpr unsigned KB = 1024;
constexpr unsigned L_XR = 0, L_WR = 64 * KB, L_CS = 148 * KB, L_XS = L_CS + 512;
constexpr unsigned L_A = 0, L_RING = 56 * KB, L_M = 152 * KB;
constexpr unsigned L_SCR = L_RING + 6 * 8 * KB;
constexpr unsigned L_RED = 56 * KB;
constexpr unsigned L_BAD = 159 * KB, L_CNT = L_BAD + 4, L_GO = L_BAD + 8;
constexpr unsigned LDS_BYTES = 160 * KB;
constexpr unsigned SPIN_CAP = 1u << 16;
constexpr unsigned CODE_PF = 35 * 1024;
constexpr unsigned OOB = 0x7f000000u;

#define WSZ(x) (x)
#define EWQ 1
#define EWM 1
#define NW1A 12
#define EWS 16
#define NW1B 18
#define NW2 16
#define NB5 5
#define MFMA(a, b, c) __builtin_amdgcn_mfma_f32_32x32x16_bf16(__builtin_bit_cast(bf16x8, a), __builtin_bit_cast(bf16x8, b), c, 0, 0, 0)
#define BAR() asm volatile("s_waitcnt lgkmcnt(0)\n\ts_barrier" ::: "memory")
#define BARX() asm volatile("s_barrier" ::: "memory")
#define LGK() asm volatile("s_waitcnt lgkmcnt(0)" ::: "memory")
#define SB() __builtin_amdgcn_sched_barrier(0)
#define PIN(x) asm volatile("" : "+v"(x))
#define EX2(x) __builtin_amdgcn_exp2f(x)
#define RLX_AGENT __ATOMIC_RELAXED, __HIP_MEMORY_SCOPE_AGENT

#define STAMP(i) do {} while (0)

template <int N> DI void waitv() { asm volatile("s_waitcnt vmcnt(%0)" ::"n"(N) : "memory"); }
DI void dma(rsrc_t rs, unsigned voff, unsigned soff, unsigned lds) {
    asm volatile("s_mov_b32 m0, %0\n\ts_nop 0\n\tbuffer_load_dwordx4 %1, %2, %3 offen lds" ::"s"(lds), "v"(voff), "s"(rs), "s"(soff) : "memory");
}
DI void dma_sc1(rsrc_t rs, unsigned voff, unsigned soff, unsigned lds) {
    asm volatile("s_mov_b32 m0, %0\n\ts_nop 0\n\tbuffer_load_dwordx4 %1, %2, %3 offen sc1 lds" ::"s"(lds), "v"(voff), "s"(rs), "s"(soff) : "memory");
}
DI u32x4 ld16(rsrc_t rs, unsigned voff, unsigned soff) { return __builtin_amdgcn_raw_buffer_load_b128(rs, voff, soff, 16); }
DI void st16(rsrc_t rs, u32x4 v, unsigned voff, unsigned soff) { __builtin_amdgcn_raw_buffer_store_b128(v, rs, voff + soff, 0, 16); }
DI void st8(rsrc_t rs, u32x2 v, unsigned voff, unsigned soff) { __builtin_amdgcn_raw_buffer_store_b64(v, rs, voff, soff, 16); }
DI u32x4 lds16(unsigned a) { return *(const LAS u32x4*)(uintptr_t)a; }
DI f32x4 lds16f(unsigned a) { return *(const LAS f32x4*)(uintptr_t)a; }
DI u32x2 ldstr(unsigned a) { return __builtin_bit_cast(u32x2, __builtin_amdgcn_ds_read_tr16_b64_v4i16((LAS s16x4*)(uintptr_t)a)); }
template <int D2> DI u32x4 wfrag(unsigned a) { u32x2 lo = ldstr(a), hi = ldstr(a + D2); return u32x4{lo[0], lo[1], hi[0], hi[1]}; }
DI unsigned pk(float lo, float hi) { return __builtin_bit_cast(unsigned, __builtin_convertvector(f32x2{lo, hi}, bf16x2)); }
DI float bflo(unsigned u) { return __uint_as_float(u << 16); }
DI float bfhi(unsigned u) { return __uint_as_float(u & 0xffff0000u); }
DI float sum32(float v) {
    auto r = __builtin_amdgcn_permlane32_swap(__float_as_uint(v), __float_as_uint(v), false, false);
    return __uint_as_float(r[0]) + __uint_as_float(r[1]);
}
DI float max32(float v) {
    auto r = __builtin_amdgcn_permlane32_swap(__float_as_uint(v), __float_as_uint(v), false, false);
    return fmaxf(__uint_as_float(r[0]), __uint_as_float(r[1]));
}
DI int fresh_lane() { int l; asm volatile("v_mbcnt_lo_u32_b32 %0, -1, 0\n\tv_mbcnt_hi_u32_b32 %0, -1, %0" : "=v"(l)); return l; }
DI float add1(float a, float b) { float d; asm("v_add_f32 %0, %1, %2" : "=v"(d) : "v"(a), "v"(b)); return d; }
DI float mul1(float a, float b) { float d; asm("v_mul_f32 %0, %1, %2" : "=v"(d) : "v"(a), "v"(b)); return d; }
DI float fma1(float a, float b, float c) { float d; asm("v_fma_f32 %0, %1, %2, %3" : "=v"(d) : "v"(a), "v"(b), "v"(c)); return d; }
DI u32x2 silu4(float a0, float a1, float a2, float a3, f32x4 c, float nm, float ce) {
    unsigned o0, o1; float z0, z1, z2, z3, t0, t1, t2, t3;
    asm("v_fma_f32 %2, %14, %10, %16\n\tv_fma_f32 %3, %14, %11, %17\n\tv_fma_f32 %4, %14, %12, %18\n\tv_fma_f32 %5, %14, %13, %19\n\t"
        "v_mul_f32 %6, %15, %2\n\tv_mul_f32 %7, %15, %3\n\tv_mul_f32 %8, %15, %4\n\tv_mul_f32 %9, %15, %5\n\t"
        "v_exp_f32 %6, %6\n\tv_exp_f32 %7, %7\n\tv_exp_f32 %8, %8\n\tv_exp_f32 %9, %9\n\t"
        "v_add_f32 %6, 1.0, %6\n\tv_add_f32 %7, 1.0, %7\n\tv_add_f32 %8, 1.0, %8\n\tv_add_f32 %9, 1.0, %9\n\t"
        "v_rcp_f32 %6, %6\n\tv_rcp_f32 %7, %7\n\tv_rcp_f32 %8, %8\n\tv_rcp_f32 %9, %9\n\t"
        "v_mul_f32 %2, %2, %6\n\tv_mul_f32 %3, %3, %7\n\tv_mul_f32 %4, %4, %8\n\tv_mul_f32 %5, %5, %9\n\t"
        "v_cvt_pk_bf16_f32 %0, %2, %3\n\tv_cvt_pk_bf16_f32 %1, %4, %5"
        : "=v"(o0), "=v"(o1), "=&v"(z0), "=&v"(z1), "=&v"(z2), "=&v"(z3), "=&v"(t0), "=&v"(t1), "=&v"(t2), "=&v"(t3)
        : "v"(c[0]), "v"(c[1]), "v"(c[2]), "v"(c[3]), "v"(nm), "v"(ce), "v"(a0), "v"(a1), "v"(a2), "v"(a3));
    return u32x2{o0, o1};
}
DI float dot2(unsigned a, unsigned b, float c) { return __builtin_amdgcn_fdot2_f32_bf16(__builtin_bit_cast(bf16x2, a), __builtin_bit_cast(bf16x2, b), c, false); }

DI void wait_flags(char* ws, unsigned off, u64 epoch) {
    const GAS u64* p = (const GAS u64*)(ws + off);
    for (unsigned sp = 0; sp < SPIN_CAP; ++sp) {
        const u64 v = __hip_atomic_load(p, RLX_AGENT);
        if (__all(v == epoch)) break;
    }
    asm volatile("" ::: "memory");
}
DI void wait_flags2(char* ws, unsigned off0, unsigned off1, u64 epoch) {
    const GAS u64* p0 = (const GAS u64*)(ws + off0); const GAS u64* p1 = (const GAS u64*)(ws + off1);
    for (unsigned sp = 0; sp < SPIN_CAP; ++sp) {
        const u64 v0 = __hip_atomic_load(p0, RLX_AGENT), v1 = __hip_atomic_load(p1, RLX_AGENT);
        if (__all(v0 == epoch && v1 == epoch)) break;
    }
    asm volatile("" ::: "memory");
}
DI void set_flag(char* ws, unsigned off, u64 epoch) { __hip_atomic_store((GAS u64*)(ws + off), epoch, RLX_AGENT); }

__global__ void __launch_bounds__(512, 2)
block_kernel(const void* __restrict__ gx, const void* __restrict__ gwqkv, const void* __restrict__ gwo, const void* __restrict__ gw1,
             const void* __restrict__ gw2, void* __restrict__ gout, char* __restrict__ ws) {
    extern __shared__ __attribute__((aligned(16))) char smem[];
    const int tid = threadIdx.x;
    const int w = __builtin_amdgcn_readfirstlane(tid >> 6);
    const int b = blockIdx.x, xcd = b & 7, bi = b >> 3;
    const rsrc_t rX = __builtin_amdgcn_make_buffer_rsrc((void*)gx, 0, SEQ * DM * 2, 0x00020000);
    const rsrc_t rWqkv = __builtin_amdgcn_make_buffer_rsrc((void*)gwqkv, 0, DM * 3 * DM * 2, 0x00020000);
    const rsrc_t rW1 = __builtin_amdgcn_make_buffer_rsrc((void*)gw1, 0, WSZ(DM * FF * 2), 0x00020000);
    const rsrc_t rW2 = __builtin_amdgcn_make_buffer_rsrc((void*)gw2, 0, WSZ(FF * DM * 2), 0x00020000);
    const rsrc_t rOut = __builtin_amdgcn_make_buffer_rsrc(gout, 0, SEQ * DM * 2, 0x00020000);
    const rsrc_t rWs = __builtin_amdgcn_make_buffer_rsrc(ws, 0, WS_END, 0x00020000);
    const int tb1 = 4 * (xcd >> 1) + bi / 7, c1 = 7 * (xcd & 1) + bi % 7;
    const int h2 = 2 * xcd + (bi & 1), qb = bi >> 1;
    const int tb3 = 2 * xcd + (bi >> 4), j3 = bi & 15, mh = j3 & 1, cb = j3 >> 1;
    const bool act3 = j3 < 14;
    const int g4 = (bi >> 4) * 2 + mh, r4 = 7 * g4;
#define R4(s) ((s) + r4 >= 28 ? (s) + r4 - 28 : (s) + r4)
    STAMP(31); STAMP(0);
    const u64 epoch = *(const __attribute__((address_space(4))) u64*)(ws + WS_EPOCH) + 1;
    {
    const int lane = tid & 63, r = lane & 31, hh = lane >> 5;
    const unsigned trn = (unsigned)((8 * hh + ((lane & 15) >> 2)) * 64 + ((lane >> 4) & 1) * 32 + (lane & 3) * 8);
    const unsigned trp = (unsigned)((4 * hh + ((lane & 15) >> 2)) * 64 + ((lane >> 4) & 1) * 32 + (lane & 3) * 8);
    const unsigned v1 = (lane >> 2) * 3584 + (lane & 3) * 16;
    const unsigned v2 = (lane >> 2) * 896 + (lane & 3) * 16;
    const unsigned lo = lane * 16;
    if (tid == 0) *(LAS u32x4*)(uintptr_t)L_BAD = u32x4{0u, 0u, 0u, 0u};

    if (w == 0) dma(rX, lane < 56 ? lane * 128 : OOB, (unsigned)b * 7168, L_M + 4096);
    if (w == 1) dma(rWqkv, lane < 37 ? lane * 128 : OOB, (unsigned)b * 4704, L_M + 4096);
    if (w == 3) dma_sc1(rWs, lane * 2097152, 0, L_M + 4096);
    if (w == 2) {
        u64 pc; asm volatile("s_getpc_b64 %0" : "=s"(pc));
        dma(__builtin_amdgcn_make_buffer_rsrc((void*)(pc & ~127ull), 0, CODE_PF, 0x00020000), lane < 10 ? lane * 128 : OOB, (unsigned)bi * 1280, L_M + 4096);
    }
    if (bi >= 28) {
        const int id = (bi - 28) * 8 + xcd, u = id * 8 + w;
        const int kind = u < 112 ? 0 : u < 224 ? 1 : 2, v = u - 112 * kind;
        const int pair = kind == 1 ? v >> 4 : v >> 2, seg = kind == 1 ? v & 15 : v & 3;
        const rsrc_t rs = __builtin_amdgcn_make_buffer_rsrc((void*)(kind == 0 ? gw1 : kind == 1 ? gw2 : gwo), 0, u >= 224 ? 0 : kind == 2 ? DM * DM * 2 : DM * FF * 2, 0x00020000);
        const unsigned rowb = kind == 0 ? 16 * 3584 : 16 * 896, nks = kind == 1 ? 112 : 28;
        const unsigned so = (unsigned)(seg * 7) * rowb + pair * 128, ld = w * 14 * KB;
#pragma unroll
        for (int i = 0; i < 7; ++i) { dma(rs, kind == 0 ? v1 : v2, so + i * rowb, ld + 2 * i * KB); dma(rs, kind == 0 ? v1 : v2, so + i * rowb + 64, ld + (2 * i + 1) * KB); }
        const unsigned dst = u >= 224 ? OOB : (kind == 0 ? WS_W1T : kind == 1 ? WS_W2T : WS_WOT) + (2 * pair * nks + seg * 7) * 1024;
        STAMP(10);
        waitv<0>();
        STAMP(11);
        float c[2] = {0.f, 0.f};
#pragma unroll
        for (int i = 0; i < 14; ++i) {
            const u32x4 f = wfrag<512>(ld + trp + i * KB);
            st16(rWs, f, lo + (i >> 1) * 1024, dst + (i & 1) * nks * 1024);
#pragma unroll
            for (int e = 0; e < 4; ++e) c[i & 1] += bflo(f[e]) + bfhi(f[e]);
        }
        STAMP(12);
        c[0] = sum32(c[0]); c[1] = sum32(c[1]);
        *(LAS float*)(uintptr_t)(L_M + w * 256 + lane * 4) = hh ? c[1] : c[0];
        BAR();
        if (id < 14 && w < 2) {
            float t = 0.f;
#pragma unroll
            for (int i = 0; i < 4; ++i) t += *(const LAS float*)(uintptr_t)(L_M + (w * 4 + i) * 256 + lane * 4);
            __builtin_amdgcn_raw_buffer_store_b32(__float_as_uint(t), rWs, lane * 4, WS_CS1 + (unsigned)(id * 2 + w) * 256, 16);
        }
        waitv<0>();
        STAMP(13);
        BAR();
        if (tid == 0) set_flag(ws, WS_FS + id * 8, epoch);
    } else {
#define ROTV 0
      const int rot = ROTV == 0 ? 0 : ROTV == 1 ? (c1 + 2 * tb1) % 7 : ((c1 + tb1) & 1);
      if (w < 4) {
        const int rin = lane >> 3, sl = lane & 7;
        const unsigned ve = rin * 896 + ((sl ^ (rin >> 1)) * 16), vo = rin * 896 + ((sl ^ (4 + (rin >> 1))) * 16);
        const unsigned xs = (unsigned)(tb1 * 128 + w * 32) * 896, xl = L_XR + w * 16 * KB;
#define ROT(c) ((c) + rot >= 7 ? (c) + rot - 7 : (c) + rot)
#define XDMA(c) do { const unsigned o_ = xs + ROT(c) * 128; _Pragma("unroll") for (int g = 0; g < 4; ++g) dma(rX, (g & 1) ? vo : ve, o_ + g * 8 * 896, xl + ((c) & 3) * 4 * KB + g * KB); } while (0)
        XDMA(0); XDMA(1);
        const unsigned xb = xl + (r >> 3) * KB + (r & 7) * 128 + ((hh ^ ((r >> 1) & 7)) * 16);
        u32x4 xf[8];
#define XRD(c) do { _Pragma("unroll") for (int s = 0; s < 4; ++s) xf[4 * ((c) & 1) + s] = lds16((xb ^ (s * 32)) + ((c) & 3) * 4 * KB); } while (0)
        f32x16 ac[3] = {{}, {}, {}};
        unsigned wbA = L_WR + trn, wbB = L_WR + trn + 63 * KB;
        PIN(wbA); PIN(wbB);
        u32x4 fw[3][3];
#define WRD(ks) do { _Pragma("unroll") for (int t = 0; t < 3; ++t) fw[(ks) % 3][t] = (ks) < 21 ? wfrag<256>(wbA + ((ks) * 3 + t) * KB) : wfrag<256>(wbB + (((ks) - 21) * 3 + t) * KB); } while (0)
        float s1 = 0.f, s2 = 0.f;
#define XST(c) do { _Pragma("unroll") for (int q = 0; q < 4; ++q) _Pragma("unroll") for (int e = 0; e < 4; ++e) { s1 = dot2(xf[4 * ((c) & 1) + q][e], 0x3F803F80u, s1); s2 = dot2(xf[4 * ((c) & 1) + q][e], xf[4 * ((c) & 1) + q][e], s2); } PIN(s1); PIN(s2); SB(); } while (0)
        waitv<4>(); XRD(0); XDMA(2); XST(0);
        BARX(); WRD(0); WRD(1); SB();
#pragma unroll
        for (int ks = 0; ks < 28; ++ks) {
            ac[0] = MFMA(fw[ks % 3][0], xf[ks & 7], ac[0]); PIN(ac[0]);
            SB();
            if (ks + 2 < 28) {
                if ((ks + 2) % 4 == 0) {
                    const int c = (ks + 2) / 4;
                    if (c < 6) waitv<4>(); else waitv<0>();
                    XRD(c);
                    if (c + 2 < 7) XDMA(c + 2);
                    BARX();
                }
                WRD(ks + 2);
                if ((ks + 2) % 4 == 1 && ks > 2) XST((ks + 2) / 4);
            }
            SB();
            ac[1] = MFMA(fw[ks % 3][1], xf[ks & 7], ac[1]); PIN(ac[1]); ac[2] = MFMA(fw[ks % 3][2], xf[ks & 7], ac[2]); PIN(ac[2]);
            SB();
        }
        STAMP(12);
        s1 = sum32(s1); s2 = sum32(s2);
        const float mean = s1 * (1.f / DM), rstd = __builtin_amdgcn_rsqf(fmaxf(s2 * (1.f / DM) - mean * mean, 0.f) + 1e-5f);
        BAR();
        STAMP(13);
        f32x4 csr[3][4];
#pragma unroll
        for (int t = 0; t < 3; ++t)
#pragma unroll
            for (int g = 0; g < 4; ++g) csr[t][g] = lds16f(L_CS + t * 128 + (8 * g + 4 * hh) * 4);
        const unsigned kt = (unsigned)(tb1 * 4 + w);
        u32x2 pkd[3][4];
#pragma unroll
        for (int t = 0; t < 3; ++t) {
            const float sc = 3 * c1 + t < 14 ? rstd * (0.18898223650461362f * 1.4426950408889634f) : rstd, ms = -mean * sc;
#pragma unroll
            for (int g = 0; g < 4; ++g) {
                float y[4];
#pragma unroll
                for (int e = 0; e < 4; ++e) y[e] = fmaf(ac[t][4 * g + e], sc, ms * csr[t][g][e]);
                pkd[t][g] = u32x2{pk(y[0], y[1]), pk(y[2], y[3])};
            }
            if (3 * c1 + t >= 28) {
#pragma unroll
                for (int g = 0; g < 4; ++g) *(LAS u32x2*)(uintptr_t)(L_SCR + (w * 3 + t) * 2 * KB + r * 64 + (2 * g + hh) * 8) = pkd[t][g];
            } else {
                const int nt = 3 * c1 + t;
                const unsigned base = WS_Q + kt * 28672 + (nt < 14 ? (unsigned)nt * 2048 : WS_K - WS_Q + (unsigned)(nt - 14) * 2048);
#pragma unroll
                for (int g = 0; g < 4; g += 2) {
                    const auto x0 = __builtin_amdgcn_permlane32_swap(pkd[t][g][0], pkd[t][g + 1][0], false, false), x1 = __builtin_amdgcn_permlane32_swap(pkd[t][g][1], pkd[t][g + 1][1], false, false);
                    st16(rWs, u32x4{x0[0], x1[0], x0[1], x1[1]}, lo + g * 512, base);
                }
            }
        }
        if (c1 >= 9) {
            LGK();
#pragma unroll
            for (int t = 0; t < 3; ++t) {
                if (3 * c1 + t >= 28) {
                    const u32x4 f0 = wfrag<512>(L_SCR + (w * 3 + t) * 2 * KB + trp), f1 = wfrag<512>(L_SCR + (w * 3 + t) * 2 * KB + trp + 1024);
                    const unsigned off = (unsigned)(hh * 7168 + ((3 * c1 + t - 28) * 32 + r) * 16);
                    st16(rWs, f0, off, WS_V + kt * 28672); st16(rWs, f1, off + 14336, WS_V + kt * 28672);
                }
            }
        }
        STAMP(14);
        waitv<0>();
        if (lane == 0 && __hip_atomic_fetch_add((LAS unsigned*)(uintptr_t)L_CNT, 1u, __ATOMIC_RELAXED, __HIP_MEMORY_SCOPE_WORKGROUP) == 3u) set_flag(ws, WS_F1 + c1 * 256 + tb1 * 8, epoch);
      } else {
        const int j = w - 4;
        const unsigned vw = (lane >> 2) * 2688 + (lane & 3) * 16;
        const unsigned so = (unsigned)(j * 16 * 2688 + c1 * 192), ld = L_WR + j * 3 * KB;
#define WDMA(st) do { const unsigned o_ = so + ROT(st) * 64 * 2688; dma(rWqkv, vw, o_, ld + (st) * 12 * KB); dma(rWqkv, vw, o_ + 64, ld + (st) * 12 * KB + KB); dma(rWqkv, vw, o_ + 128, ld + (st) * 12 * KB + 2 * KB); } while (0)
        WDMA(0); WDMA(1);
        const u32x4 ones = {0x3F803F80u, 0x3F803F80u, 0x3F803F80u, 0x3F803F80u};
        f32x16 cacc = {};
        unsigned wb = L_WR + trn + (j < 3 ? j : 0) * KB;
#pragma unroll
        for (int st = 0; st < 7; ++st) {
            if (st < 5) { waitv<3>(); WDMA(st + 2); } else if (st == 5) waitv<4>(); else waitv<1>();
            if (st == 4) dma(__builtin_amdgcn_make_buffer_rsrc((void*)gwo, 0, DM * DM * 2, 0x00020000), lane < 14 ? lane * 128 : OOB, j == 3 ? (unsigned)(bi * 8 + xcd) * 1792 : OOB, L_M + 4096);
            BARX();
            if (j < 3) {
                u32x4 f[4];
#pragma unroll
                for (int q = 0; q < 4; ++q) f[q] = wfrag<256>(wb + q * 3 * KB);
#pragma unroll
                for (int q = 0; q < 4; ++q) cacc = MFMA(ones, f[q], cacc);
                wb += 12 * KB; PIN(wb); PIN(cacc);
            }
            if (st == 0) STAMP(10); if (st == 3) STAMP(11);
        }
        if (j < 3 && hh == 0) *(LAS float*)(uintptr_t)(L_CS + j * 128 + r * 4) = cacc[0];
        BAR();
        while (*(const volatile LAS unsigned*)(uintptr_t)L_CNT < 4u) __builtin_amdgcn_s_sleep(1);
      }
    }
    STAMP(1); STAMP(25);

    {
        const int qh = w & 1, kq = w >> 1;
        __builtin_amdgcn_s_setprio(3);
        const unsigned ch = (unsigned)((28 * h2) >> 3) * 512;
        const unsigned qso = WS_Q + (unsigned)(qb * 4 + qh * 2) * 28672 + ch;
        const rsrc_t rK = __builtin_amdgcn_make_buffer_rsrc(ws + WS_K + (unsigned)kq * 16 * 28672 + ch, 0, 15 * 28672 + 2048, 0x00020000);
        const rsrc_t rV = __builtin_amdgcn_make_buffer_rsrc(ws + WS_V + (unsigned)kq * 16 * 28672 + (unsigned)h2 * 448, 0, 16 * 28672 - h2 * 448, 0x00020000);
        unsigned vo0 = (unsigned)(hh * 7168 + r * 16), vo1 = vo0 + 14336;
        const unsigned vpad = r == DH ? 0x3F803F80u : 0u;
        unsigned qm0 = ((h2 & 1) && !hh) ? 0u : ~0u, qm1 = (!(h2 & 1) && hh) ? 0u : ~0u;
        u32x4 kf[4][2], vf[4][2], pa[2], pb[2], qa0, qa1, qb0, qb1;
        f32x16 oa = {}, ob = {}, sa, sb = {}, ca, cbv;
#pragma unroll
        for (int i = 0; i < 4; ++i) { vf[i][0] = vf[i][1] = u32x4{vpad, vpad, vpad, vpad}; PIN(vf[i][0]); PIN(vf[i][1]); }
        pb[0] = pb[1] = u32x4{0u, 0u, 0u, 0u};
        PIN(oa); PIN(ob); PIN(sb); PIN(pb[0]); PIN(pb[1]); PIN(vo0); PIN(vo1); PIN(qm0); PIN(qm1);
        SB();
        if (w == 0) {
            const int c0 = (28 * h2) & ~7;
            const int col = lane < 32 ? 448 + c0 + 31 * ((lane >> 4) & 1) : 896 + 28 * h2 + 27 * ((lane >> 4) & 1);
            wait_flags2(ws, WS_F1 + (unsigned)(col / 96) * 256 + (lane & 15) * 8, WS_F1 + (unsigned)((c0 + 31 * (lane & 1)) / 96) * 256 + qb * 8, epoch);
            *(volatile LAS unsigned*)(uintptr_t)L_GO = 1u;
        } else {
            if (w >= 4 && act3) {
                const rsrc_t rWo = __builtin_amdgcn_make_buffer_rsrc((void*)gwo, 0, DM * DM * 2, 0x00020000);
                __builtin_amdgcn_s_sleep(EWS);
#pragma unroll
                for (int i = 0; i < 7; ++i) { const int id = 4 * i + w - 4; dma(rWo, v2, (unsigned)(id * 16 * 896 + cb * 128), L_A + id * 2 * KB); dma(rWo, v2, (unsigned)(id * 16 * 896 + cb * 128 + 64), L_A + id * 2 * KB + KB); }
            }
            while (*(const volatile LAS unsigned*)(uintptr_t)L_GO == 0u) __builtin_amdgcn_s_sleep(1);
            asm volatile("" ::: "memory");
        }
        STAMP(2); STAMP(26);
        asm volatile("buffer_load_dwordx4 %0, %6, %7, %8 offen sc1\n\tbuffer_load_dwordx4 %1, %6, %7, %8 offen offset:1024 sc1\n\t"
                     "buffer_load_dwordx4 %4, %6, %7, %10 offen sc1\n\tbuffer_load_dwordx4 %5, %6, %7, %10 offen offset:1024 sc1\n\t"
                     "buffer_load_dwordx4 %2, %6, %7, %9 offen sc1\n\tbuffer_load_dwordx4 %3, %6, %7, %9 offen offset:1024 sc1"
                     : "=&v"(qa0), "=&v"(qa1), "=&v"(qb0), "=&v"(qb1), "=&v"(pa[0]), "=&v"(pa[1]) : "v"(lo), "s"(rWs), "s"(qso), "s"(qso + 28672), "s"(WS_K + ch) : "memory");
#define KVLD(KC, K0, K1, V0, V1, SK, SV) asm volatile("buffer_load_dwordx4 %0, %4, %7, %9 offen sc1\n\tbuffer_load_dwordx4 %1, %4, %7, %9 offen offset:1024 sc1\n\t" \
                "s_mov_b64 exec, %11\n\tbuffer_load_dwordx4 %2, %5, %8, %10 offen sc1\n\tbuffer_load_dwordx4 %3, %6, %8, %10 offen sc1\n\ts_mov_b64 exec, -1" \
                : KC(K0), KC(K1), "+v"(V0), "+v"(V1) : "v"(lo), "v"(vo0), "v"(vo1), "s"(rK), "s"(rV), "s"(SK), "s"(SV), "s"(0x0fffffff0fffffffull) : "memory")
#pragma unroll
        for (int i = 0; i < 4; ++i) KVLD("=&v", kf[i][0], kf[i][1], vf[(i + 3) & 3][0], vf[(i + 3) & 3][1], (unsigned)(i * 28672), i ? (unsigned)((i - 1) * 28672) : OOB);
        asm volatile("s_waitcnt vmcnt(16)" : "+v"(qa0), "+v"(qa1), "+v"(qb0), "+v"(qb1), "+v"(pa[0]), "+v"(pa[1]) :: "memory");
        qa0[0] &= qm0; qa0[1] &= qm0; qb0[0] &= qm0; qb0[1] &= qm0; qa1[2] &= qm1; qa1[3] &= qm1; qb1[2] &= qm1; qb1[3] &= qm1;
        {
            const f32x16 z = {};
            ca = MFMA(pa[0], qa0, z); cbv = MFMA(pa[0], qb0, z); ca = MFMA(pa[1], qa1, ca); cbv = MFMA(pa[1], qb1, cbv);
            float ma = ca[0], mb = cbv[0];
#pragma unroll
            for (int i = 1; i < 16; ++i) { ma = fmaxf(ma, ca[i]); mb = fmaxf(mb, cbv[i]); }
            ma = -max32(ma); mb = -max32(mb);
#pragma unroll
            for (int i = 0; i < 16; ++i) { ca[i] = ma; cbv[i] = mb; }
        }
        PIN(ca); PIN(cbv);
        asm volatile("s_waitcnt vmcnt(12)" : "+v"(kf[0][0]), "+v"(kf[0][1]), "+v"(vf[3][0]), "+v"(vf[3][1]) :: "memory");
        sa = MFMA(kf[0][0], qa0, ca); sa = MFMA(kf[0][1], qa1, sa);
        PIN(sa); STAMP(15);
        SB();
#define GRP(MF, S, i, T, j, P) do { MF; SB(); \
        S[4 * (i)] = EX2(S[4 * (i)]); S[4 * (i) + 1] = EX2(S[4 * (i) + 1]); S[4 * (i) + 2] = EX2(S[4 * (i) + 2]); S[4 * (i) + 3] = EX2(S[4 * (i) + 3]); SB(); \
        P[(j) >> 1][((j) & 1) * 2] = pk(T[4 * (j)], T[4 * (j) + 1]); P[(j) >> 1][((j) & 1) * 2 + 1] = pk(T[4 * (j) + 2], T[4 * (j) + 3]); PIN(P[(j) >> 1]); PIN(S); SB(); } while (0)
#pragma unroll 1
        for (int tq = 0; tq < 4; ++tq) {
            const unsigned sk = (unsigned)tq * 4 * 28672;
            if (tq == 1) __builtin_amdgcn_s_setprio(2); if (tq == 2) __builtin_amdgcn_s_setprio(1); if (tq == 3) __builtin_amdgcn_s_setprio(0);
#pragma unroll
            for (int u = 0; u < 4; ++u) {
                GRP(ob = MFMA(vf[(u + 3) & 3][0], pb[0], ob), sa, 0, sb, 3, pb);
                GRP(sb = MFMA(kf[u][0], qb0, cbv), sa, 1, sa, 0, pa);
                GRP(sb = MFMA(kf[u][1], qb1, sb), sa, 2, sa, 1, pa);
                GRP(ob = MFMA(vf[(u + 3) & 3][1], pb[1], ob), sa, 3, sa, 2, pa);
                KVLD("+v", kf[u][0], kf[u][1], vf[(u + 3) & 3][0], vf[(u + 3) & 3][1], sk + (u + 4) * 28672, sk + (u + 3) * 28672);
                asm volatile("s_waitcnt vmcnt(12)" : "+v"(kf[(u + 1) & 3][0]), "+v"(kf[(u + 1) & 3][1]), "+v"(vf[u][0]), "+v"(vf[u][1]) :: "memory");
                SB();
                GRP(oa = MFMA(vf[u][0], pa[0], oa), sb, 0, sa, 3, pa);
                GRP(sa = MFMA(kf[(u + 1) & 3][0], qa0, ca), sb, 1, sb, 0, pb);
                GRP(sa = MFMA(kf[(u + 1) & 3][1], qa1, sa), sb, 2, sb, 1, pb);
                GRP(oa = MFMA(vf[u][1], pa[1], oa), sb, 3, sb, 2, pb);
            }
        }
        asm volatile("s_waitcnt vmcnt(0)" : "+v"(kf[0][0]), "+v"(kf[0][1]), "+v"(kf[1][0]), "+v"(kf[1][1]), "+v"(kf[2][0]), "+v"(kf[2][1]), "+v"(kf[3][0]), "+v"(kf[3][1]),
                     "+v"(vf[0][0]), "+v"(vf[0][1]), "+v"(vf[1][0]), "+v"(vf[1][1]), "+v"(vf[2][0]), "+v"(vf[2][1]) :: "memory");
        pb[1][2] = pk(sb[12], sb[13]); pb[1][3] = pk(sb[14], sb[15]);
        ob = MFMA(vf[3][0], pb[0], ob); ob = MFMA(vf[3][1], pb[1], ob);
        PIN(ob); STAMP(16);
        const int lane = fresh_lane(), r = lane & 31, hh = lane >> 5;
        if (w == 0) wait_flags(ws, WS_FS + (unsigned)r * 8, epoch);
        const unsigned lo = lane * 16;
        {
            float t = 0.f;
#pragma unroll
            for (int i = 0; i < 16; ++i) t += fabsf(oa[i]) + fabsf(ob[i]);
            if (__any(!(t < 1.0e36f))) *(LAS unsigned*)(uintptr_t)L_BAD = 1;
        }
        unsigned so0, so1;
        { const int m = 4 * (kq & 1) + hh, g = 7 * h2 + m, g2 = g + 2;
          so0 = (unsigned)(((g >> 2) * 64 + (g & 1) * 32 + r) * 16 + ((g >> 1) & 1) * 8); so1 = m + 2 < 7 ? (unsigned)(((g2 >> 2) * 64 + (g2 & 1) * 32 + r) * 16 + ((g2 >> 1) & 1) * 8) : OOB; }
        float o8[8], l;
#pragma unroll 1
        for (int pass = 0; pass < 2; ++pass) {
            if (hh) { *(LAS float*)(uintptr_t)(L_M + ((kq * 2 + qh) * 2) * 128 + r * 4) = oa[12]; *(LAS float*)(uintptr_t)(L_M + ((kq * 2 + qh) * 2 + 1) * 128 + r * 4) = ob[12]; }
#pragma unroll
            for (int p = 0; p < 4; ++p) {
                if (p != kq) {
                    const unsigned dst = L_SCR + (unsigned)((qh * 4 + p) * 3 + (kq > p ? kq - 1 : kq)) * 2048 + lo;
                    const f32x16& o = (p >> 1) ? ob : oa;
                    *(LAS f32x4*)(uintptr_t)dst = f32x4{o[8 * (p & 1)], o[8 * (p & 1) + 1], o[8 * (p & 1) + 2], o[8 * (p & 1) + 3]};
                    *(LAS f32x4*)(uintptr_t)(dst + 1024) = f32x4{o[8 * (p & 1) + 4], o[8 * (p & 1) + 5], o[8 * (p & 1) + 6], o[8 * (p & 1) + 7]};
                }
            }
#pragma unroll
            for (int i = 0; i < 8; ++i) { const float lo8 = (kq & 2) ? ob[i] : oa[i], hi8 = (kq & 2) ? ob[8 + i] : oa[8 + i]; o8[i] = (kq & 1) ? hi8 : lo8; PIN(o8[i]); }
            PIN(so0); PIN(so1);
            BAR();
            STAMP(22);
            const unsigned badw = *(const volatile LAS unsigned*)(uintptr_t)L_BAD;
            l = 0.f;
#pragma unroll
            for (int i = 0; i < 4; ++i) l += *(const LAS float*)(uintptr_t)(L_M + ((i * 2 + qh) * 2 + (kq >> 1)) * 128 + r * 4);
#pragma unroll
            for (int i = 0; i < 3; ++i) {
                const unsigned src = L_SCR + (unsigned)((qh * 4 + kq) * 3 + i) * 2048 + lo;
                const f32x4 a = lds16f(src), c = lds16f(src + 1024);
#pragma unroll
                for (int e = 0; e < 4; ++e) { o8[e] += a[e]; o8[4 + e] += c[e]; }
            }
            if (pass == 1 || !__builtin_amdgcn_readfirstlane(badw)) break;
            const f32x16 z = {};
            float ma = -INFINITY, mb = -INFINITY;
#pragma unroll 1
            for (int t = 0; t < 16; ++t) {
                const u32x4 k0 = ld16(rK, lo, t * 28672), k1 = ld16(rK, lo + 1024, t * 28672);
                f32x16 ta = MFMA(k0, qa0, z), tb = MFMA(k0, qb0, z); ta = MFMA(k1, qa1, ta); tb = MFMA(k1, qb1, tb);
#pragma unroll
                for (int i = 0; i < 16; ++i) { ma = fmaxf(ma, ta[i]); mb = fmaxf(mb, tb[i]); }
            }
            ma = max32(ma); mb = max32(mb);
            *(LAS float*)(uintptr_t)(L_M + 2048 + ((kq * 2 + qh) * 2 + hh) * 128 + r * 4) = hh ? mb : ma;
            BAR();
#pragma unroll
            for (int i = 0; i < 4; ++i) { ma = fmaxf(ma, *(const LAS float*)(uintptr_t)(L_M + 2048 + ((i * 2 + qh) * 2) * 128 + r * 4)); mb = fmaxf(mb, *(const LAS float*)(uintptr_t)(L_M + 2048 + ((i * 2 + qh) * 2 + 1) * 128 + r * 4)); }
            oa = z; ob = z;
            const unsigned vp = r == DH ? 0x3F803F80u : 0u, vm = r < DH ? ~0u : 0u, vo = (unsigned)(hh * 7168 + (r < DH ? r : 0) * 16);
#pragma unroll 1
            for (int t = 0; t < 16; ++t) {
                const u32x4 k0 = ld16(rK, lo, t * 28672), k1 = ld16(rK, lo + 1024, t * 28672);
                const u32x4 v0 = (ld16(rV, vo, t * 28672) & vm) | vp, v1 = (ld16(rV, vo + 14336, t * 28672) & vm) | vp;
                f32x16 ta = MFMA(k0, qa0, z), tb = MFMA(k0, qb0, z); ta = MFMA(k1, qa1, ta); tb = MFMA(k1, qb1, tb);
                u32x4 p0, p1, p2, p3;
#pragma unroll
                for (int i = 0; i < 4; ++i) {
                    p0[i] = pk(EX2(ta[2 * i] - ma), EX2(ta[2 * i + 1] - ma)); p1[i] = pk(EX2(ta[8 + 2 * i] - ma), EX2(ta[9 + 2 * i] - ma));
                    p2[i] = pk(EX2(tb[2 * i] - mb), EX2(tb[2 * i + 1] - mb)); p3[i] = pk(EX2(tb[8 + 2 * i] - mb), EX2(tb[9 + 2 * i] - mb));
                }
                oa = MFMA(v0, p0, oa); oa = MFMA(v1, p1, oa); ob = MFMA(v0, p2, ob); ob = MFMA(v1, p3, ob);
            }
        }
        const float il = __builtin_amdgcn_rcpf(l);
        const unsigned ob_ = WS_O + (unsigned)(qb * 4 + qh * 2 + (kq >> 1)) * 28 * 1024;
        st8(rWs, u32x2{pk(o8[0] * il, o8[1] * il), pk(o8[2] * il, o8[3] * il)}, so0, ob_);
        st8(rWs, u32x2{pk(o8[4] * il, o8[5] * il), pk(o8[6] * il, o8[7] * il)}, so1, ob_);
    }
    }
    const int lane = fresh_lane(), r = lane & 31, hh = lane >> 5;
    const unsigned lo = lane * 16;
    const unsigned trp = (unsigned)((4 * hh + ((lane & 15) >> 2)) * 64 + ((lane >> 4) & 1) * 32 + (lane & 3) * 8);
    if (!act3) {
        waitv<0>();
        BAR();
        if (tid == 0) set_flag(ws, WS_F2 + qb * 256 + h2 * 8, epoch);
        if (b == 14 * 8 && w == 0) {
#pragma unroll 1
            for (int i = 0; i < 4; ++i) wait_flags(ws, WS_F2 + (unsigned)((i * 4 + (lane >> 4)) * 256 + (lane & 15) * 8), epoch);
            if (lane == 0) set_flag(ws, WS_EPOCH, epoch);
        }
        return;
    }

    const int mt = w & 1, kq = w >> 1;
    const unsigned tt0 = (unsigned)(tb3 * 4 + mh * 2), grp = (unsigned)(tb3 * 2 + mh);
    const unsigned xo = ((tt0 + mt) * 32 + r) * 896 + (cb * 64 + 16 * kq + 4 * hh) * 2;
    const unsigned w1b = WS_W1T + (unsigned)(cb * 8 + w) * 28 * 1024;
    u32x4 fr[28];
    u32x4 hres;
    f32x4 cs[4];
    {
        u32x4 fa[14];
#pragma unroll
        for (int i = 0; i < 14; ++i) fa[i] = wfrag<512>(L_A + trp + (kq * 14 + i) * KB);
#pragma unroll
        for (int i = 0; i < 14; ++i) PIN(fa[i]);
        waitv<0>();
        BAR();
        STAMP(3); STAMP(27);
        if (tid == 0) set_flag(ws, WS_F2 + qb * 256 + h2 * 8, epoch);
        SB();
        const u32x2 xr0 = __builtin_amdgcn_raw_buffer_load_b64(rX, xo, 0, 0), xr1 = __builtin_amdgcn_raw_buffer_load_b64(rX, xo + 16, 0, 0);
#pragma unroll
        for (int g = 0; g < 4; ++g) cs[g] = __builtin_bit_cast(f32x4, ld16(rWs, (8 * g + 4 * hh) * 4, WS_CS1 + (unsigned)(cb * 256 + w * 32) * 4));
#pragma unroll
        for (int i = 0; i < NW1A; ++i) fr[i] = ld16(rWs, lo, w1b + R4(i) * 1024);
        if (w == 0) wait_flags(ws, WS_F2 + tb3 * 256 + (lane & 15) * 8, epoch);
        BARX();
        STAMP(4); STAMP(28);
        u32x4 bf[7];
#pragma unroll
        for (int i = 0; i < 7; ++i) bf[i] = ld16(rWs, lo, WS_O + ((tt0 + mt) * 28 + 7 * kq + i) * 1024);
        SB();
        f32x16 a0 = {}, a1 = {};
#pragma unroll
        for (int i = 0; i < 7; ++i) { a0 = MFMA(fa[2 * i], bf[i], a0); a1 = MFMA(fa[2 * i + 1], bf[i], a1); }
        PIN(a0); PIN(a1);
        SB();
#pragma unroll
        for (int i = NW1A; i < NW1B; ++i) fr[i] = ld16(rWs, lo, w1b + R4(i) * 1024);
        SB();
        STAMP(23);
        float o8[8];
#pragma unroll
        for (int p = 0; p < 4; ++p) {
            if (p != kq) {
                const unsigned dst = L_RED + (unsigned)((mt * 4 + p) * 3 + (kq > p ? kq - 1 : kq)) * 2048 + lo; const f32x16& o = (p >> 1) ? a1 : a0;
                *(LAS f32x4*)(uintptr_t)dst = f32x4{o[8 * (p & 1)], o[8 * (p & 1) + 1], o[8 * (p & 1) + 2], o[8 * (p & 1) + 3]};
                *(LAS f32x4*)(uintptr_t)(dst + 1024) = f32x4{o[8 * (p & 1) + 4], o[8 * (p & 1) + 5], o[8 * (p & 1) + 6], o[8 * (p & 1) + 7]};
            }
        }
#pragma unroll
        for (int i = 0; i < 8; ++i) { const float lo8 = (kq & 2) ? a1[i] : a0[i], hi8 = (kq & 2) ? a1[8 + i] : a0[8 + i]; o8[i] = (kq & 1) ? hi8 : lo8; }
        o8[0] += bflo(xr0[0]); o8[1] += bfhi(xr0[0]); o8[2] += bflo(xr0[1]); o8[3] += bfhi(xr0[1]); o8[4] += bflo(xr1[0]); o8[5] += bfhi(xr1[0]); o8[6] += bflo(xr1[1]); o8[7] += bfhi(xr1[1]);
#pragma unroll
        for (int i = 0; i < 8; ++i) PIN(o8[i]);
        BAR();
        STAMP(24);
#pragma unroll
        for (int i = 0; i < 3; ++i) {
            const unsigned src = L_RED + (unsigned)((mt * 4 + kq) * 3 + i) * 2048 + lo;
            const f32x4 a = lds16f(src), c = lds16f(src + 1024);
#pragma unroll
            for (int e = 0; e < 4; ++e) { o8[e] += a[e]; o8[4 + e] += c[e]; }
        }
        hres = u32x4{pk(o8[0], o8[1]), pk(o8[2], o8[3]), pk(o8[4], o8[5]), pk(o8[6], o8[7])};
        st16(rWs, hres, lo, WS_H + ((tt0 + mt) * 28 + cb * 4 + kq) * 1024);
        float s1 = 0.f, s2 = 0.f;
#pragma unroll
        for (int e = 0; e < 4; ++e) { const float a = bflo(hres[e]), c = bfhi(hres[e]); s1 += a + c; s2 = fmaf(a, a, fmaf(c, c, s2)); }
        s1 = sum32(s1); s2 = sum32(s2);
        st8(rWs, u32x2{__float_as_uint(s1), __float_as_uint(s2)}, hh ? OOB : r * 8, WS_ST + ((tt0 + mt) * 28 + cb * 4 + kq) * 256);
        waitv<0>();
    }
    STAMP(5);
    float rstd2;
    u32x4 f2[28];
    const int r5 = 2 * ((cb + 2 * g4) % 7);
    const unsigned w2b = WS_W2T + (unsigned)(cb * 2 * 112 + 14 * w) * 1024;
#define R5(t) ((t) + r5 >= 14 ? (t) + r5 - 14 : (t) + r5)
    {
        BARX();
        if (tid == 0) set_flag(ws, WS_F3 + grp * 256 + cb * 8, epoch);
        SB();
#pragma unroll
        for (int i = NW1B; i < 28; ++i) fr[i] = ld16(rWs, lo, w1b + R4(i) * 1024);
#pragma unroll
        for (int i = 0; i < 28; ++i) PIN(fr[i]);
        u32x2 stv[7];
        if (w < 4) {
            if (w == 0) {
                wait_flags(ws, WS_F3 + grp * 256 + (unsigned)(lane % 7) * 8, epoch); *(volatile LAS unsigned*)(uintptr_t)L_GO = 3u;
            } else { while (*(const volatile LAS unsigned*)(uintptr_t)L_GO != 3u) __builtin_amdgcn_s_sleep(1); asm volatile("" ::: "memory"); }
            STAMP(6);
#pragma unroll
            for (int i = 0; i < 14; ++i) dma_sc1(rWs, lo, WS_H + ((tt0 + (w & 1)) * 28 + R4(2 * i + (w >> 1))) * 1024, L_A + (4 * i + w) * KB);
            waitv<7>();
        } else {
            while (*(const volatile LAS unsigned*)(uintptr_t)L_GO != 3u) __builtin_amdgcn_s_sleep(1);
            asm volatile("" ::: "memory");
            STAMP(6);
#pragma unroll
            for (int i = 0; i < 7; ++i) stv[i] = __builtin_amdgcn_raw_buffer_load_b64(rWs, (unsigned)(hh * 256 + r * 8), WS_ST + ((tt0 + (w & 1)) * 28 + 4 * i + (w & 2)) * 256, 16);
        }
        BARX();
        STAMP(17);
        f32x16 a0 = {}, a1 = {};
        u32x4 b0[3], b1[3];
        b0[0] = lds16(L_A + lo); b1[0] = lds16(L_A + KB + lo); b0[1] = lds16(L_A + 2 * KB + lo); b1[1] = lds16(L_A + 3 * KB + lo);
        SB();
        f32x2 sv[2][4];
        float mean[2], rs[2];
#pragma unroll
        for (int s = 0; s < 28; ++s) {
            if (s == 12) {
                if (w >= 4) {
                    float s1 = 0.f, s2 = 0.f;
#pragma unroll
                    for (int i = 0; i < 7; ++i) { s1 = add1(s1, __uint_as_float(stv[i][0])); s2 = add1(s2, __uint_as_float(stv[i][1])); }
                    *(LAS f32x2*)(uintptr_t)(L_M + (w - 4) * 512 + lane * 8) = f32x2{s1, s2};
                } else waitv<12>();
                BAR();
            }
            if (s + 2 < 28) { const int n = s + 2; b0[n % 3] = lds16(L_A + (2 * n) * KB + lo); b1[n % 3] = lds16(L_A + (2 * n + 1) * KB + lo); }
            if (s == 13 || s == 14) {
#pragma unroll
                for (int i = 0; i < 4; ++i) sv[s - 13][i] = *(const LAS f32x2*)(uintptr_t)(L_M + ((i >> 1) * 2 + (s - 13)) * 512 + ((i & 1) * 32 + r) * 8);
            }
            SB();
            a0 = MFMA(fr[s], b0[s % 3], a0); a1 = MFMA(fr[s], b1[s % 3], a1);
            PIN(a0); PIN(a1);
            SB();
            if ((s + 1) * NW2 / 28 > s * NW2 / 28) { const int q = s * NW2 / 28; f2[q] = ld16(rWs, lo, w2b + (unsigned)((q & 1) * 112 + R5(q >> 1)) * 1024); }
            if (s == 17 || s == 18) {
                const int m = s - 17;
                const float s1 = add1(add1(sv[m][0][0], sv[m][1][0]), add1(sv[m][2][0], sv[m][3][0])), s2 = add1(add1(sv[m][0][1], sv[m][1][1]), add1(sv[m][2][1], sv[m][3][1]));
                mean[m] = s1 * (1.f / DM); rs[m] = __builtin_amdgcn_rsqf(fmaxf(s2 * (1.f / DM) - mean[m] * mean[m], 0.f) + 1e-5f);
                PIN(mean[m]); PIN(rs[m]);
            }
            SB();
        }
        rstd2 = mt ? rs[1] : rs[0];
        asm volatile("s_nop 15" : "+v"(a0), "+v"(a1));
        STAMP(18);
#pragma unroll
        for (int m = 0; m < 2; ++m) {
            const f32x16& a = m ? a1 : a0;
            const float ce = rs[m] * -1.4426950408889634f, nm = -mean[m];
            u32x4 o[2];
#pragma unroll
            for (int g = 0; g < 4; ++g) {
                const u32x2 y = silu4(a[4 * g], a[4 * g + 1], a[4 * g + 2], a[4 * g + 3], cs[g], nm, ce);
                o[g >> 1][(g & 1) * 2] = y[0]; o[g >> 1][(g & 1) * 2 + 1] = y[1];
            }
            const unsigned so = WS_H1 + ((tt0 + m) * 112 + cb * 16 + w * 2) * 1024;
            st16(rWs, o[0], lo, so); st16(rWs, o[1], lo + 1024, so);
        }
        STAMP(19);
        waitv<0>();
    }
    STAMP(7);
    {
        BAR();
        if (tid == 0) set_flag(ws, WS_F4 + grp * 256 + cb * 8, epoch);
        SB();
#pragma unroll
        for (int q = NW2; q < 28; ++q) f2[q] = ld16(rWs, lo, w2b + (unsigned)((q & 1) * 112 + R5(q >> 1)) * 1024);
        if (w == 0) wait_flags(ws, WS_F4 + grp * 256 + (unsigned)(lane % 7) * 8, epoch);
        BARX();
        STAMP(8);
        u32x4 bf[28];
        const unsigned bs = WS_H1 + (tt0 * 112 + 14 * w) * 1024;
#define BLD(t) do { const unsigned o_ = bs + R5(t) * 1024; bf[2 * (t)] = ld16(rWs, lo, o_); bf[2 * (t) + 1] = ld16(rWs, lo, o_ + 112 * 1024); } while (0)
#pragma unroll
        for (int t = 0; t < NB5; ++t) BLD(t);
        SB();
        f32x16 ac[4] = {{}, {}, {}, {}};
#pragma unroll
        for (int t = 0; t < 14; ++t) {
            ac[0] = MFMA(f2[2 * t], bf[2 * t], ac[0]); ac[2] = MFMA(f2[2 * t + 1], bf[2 * t], ac[2]);
            ac[1] = MFMA(f2[2 * t], bf[2 * t + 1], ac[1]); ac[3] = MFMA(f2[2 * t + 1], bf[2 * t + 1], ac[3]);
            PIN(ac[0]); PIN(ac[1]); PIN(ac[2]); PIN(ac[3]);
            SB();
            if (t + NB5 < 14) BLD(t + NB5);
            SB();
            if (t == 0) STAMP(20); if (t == 13) STAMP(21);
        }
#pragma unroll
        for (int p = 0; p < 8; ++p) {
            const f32x16& o = ac[(p >> 2) * 2 + (p & 1)]; const int h8 = ((p >> 1) & 1) * 8;
            const unsigned dst = (unsigned)(p * 8 + w) * 2048 + lo;
            *(LAS f32x4*)(uintptr_t)dst = f32x4{o[h8], o[h8 + 1], o[h8 + 2], o[h8 + 3]};
            *(LAS f32x4*)(uintptr_t)(dst + 1024) = f32x4{o[h8 + 4], o[h8 + 5], o[h8 + 6], o[h8 + 7]};
        }
        BAR();
        f32x4 y0 = {0.f, 0.f, 0.f, 0.f}, y1 = y0;
#pragma unroll
        for (int i = 0; i < 8; ++i) { const unsigned src = (unsigned)(w * 8 + i) * 2048 + lo; y0 += lds16f(src); y1 += lds16f(src + 1024); }
        const u32x2 z0 = {pk(fmaf(y0[0], rstd2, bflo(hres[0])), fmaf(y0[1], rstd2, bfhi(hres[0]))), pk(fmaf(y0[2], rstd2, bflo(hres[1])), fmaf(y0[3], rstd2, bfhi(hres[1])))};
        const u32x2 z1 = {pk(fmaf(y1[0], rstd2, bflo(hres[2])), fmaf(y1[1], rstd2, bfhi(hres[2]))), pk(fmaf(y1[2], rstd2, bflo(hres[3])), fmaf(y1[3], rstd2, bfhi(hres[3])))};
        __builtin_amdgcn_raw_buffer_store_b64(z0, rOut, xo, 0, 0);
        __builtin_amdgcn_raw_buffer_store_b64(z1, rOut, xo + 16, 0, 0);
    }
    STAMP(9); STAMP(30);
}

extern "C" void kernel_launch(void* const* d_in, const int* in_sizes, int n_in, void* d_out, int out_size, void* d_ws, size_t ws_size,
                              hipStream_t stream) {
    (void)hipFuncSetAttribute((const void*)block_kernel, hipFuncAttributeMaxDynamicSharedMemorySize, LDS_BYTES);
    block_kernel<<<256, 512, LDS_BYTES, stream>>>(d_in[0], d_in[1], d_in[2], d_in[4], d_in[5], d_out, (char*)d_ws);
}
```

```cpp
#include <hip/hip_runtime.h>
#include <stdint.h>

typedef __attribute__((ext_vector_type(8))) __bf16 bf16x8;
typedef __attribute__((ext_vector_type(2))) __bf16 bf16x2;
typedef __attribute__((ext_vector_type(4))) short s16x4;
typedef __attribute__((ext_vector_type(16))) float f32x16;
typedef __attribute__((ext_vector_type(4))) float f32x4;
typedef __attribute__((ext_vector_type(2))) float f32x2;
typedef __attribute__((ext_vector_type(4))) unsigned u32x4;
typedef __attribute__((ext_vector_type(2))) unsigned u32x2;
typedef unsigned long long u64;
#define LAS __attribute__((address_space(3)))
#define GAS __attribute__((address_space(1)))
#define DI __device__ __forceinline__
typedef __amdgpu_buffer_rsrc_t rsrc_t;

constexpr int SEQ = 2048, DM = 448, NH = 16, DH = 28, FF = 1792;
constexpr unsigned WS_EPOCH = 0;
constexpr unsigned WS_F1 = 4096;
constexpr unsigned WS_FS = WS_F1 + 16 * 256;
constexpr unsigned WS_F2 = WS_FS + 1024;
constexpr unsigned WS_F3 = WS_F2 + 16 * 256;
constexpr unsigned WS_F4 = WS_F3 + 32 * 256;
constexpr unsigned WS_CS0 = 65536;
constexpr unsigned WS_CS1 = WS_CS0 + 21 * 256;
constexpr unsigned WS_STAMP = 32u << 20;
constexpr unsigned WS_Q = 131072;
constexpr unsigned WS_K = WS_Q + 64 * 28 * 1024;
constexpr unsigned WS_V = WS_K + 64 * 28 * 1024;
constexpr unsigned WS_O = WS_V + 64 * 28 * 1024;
constexpr unsigned WS_H = WS_O + 64 * 32 * 1024;
constexpr unsigned WS_ST = WS_H + 64 * 28 * 1024;
constexpr unsigned WS_H1 = WS_ST + 64 * 28 * 256;
constexpr unsigned WS_W1T = WS_H1 + 64 * 112 * 1024;
constexpr unsigned WS_W2T = WS_W1T + 56 * 28 * 1024;
constexpr unsigned WS_WOT = WS_W2T + 14 * 112 * 1024;
constexpr unsigned WS_END = WS_WOT + 14 * 28 * 1024;
constexpr unsigned KB = 1024;
constexpr unsigned L_XR = 0, L_WR = 64 * KB, L_CS = 148 * KB, L_XS = L_CS + 512;
constexpr unsigned L_A = 0, L_RING = 56 * KB, L_M = 152 * KB;
constexpr unsigned L_SCR = L_RING + 6 * 8 * KB;
constexpr unsigned L_CMB = 64 * KB;
constexpr unsigned L_RED = 64 * KB;
constexpr unsigned L_BAD = 159 * KB, L_CNT = L_BAD + 4, L_GO = L_BAD + 8;
constexpr unsigned LDS_BYTES = 160 * KB;
constexpr unsigned SPIN_CAP = 1u << 16;
constexpr unsigned CODE_PF = 32 * 1024;
constexpr unsigned OOB = 0x7f000000u;

#define WSZ(x) (x)
#define EWQ 1
#define EWM 1
#define NW1A 12
#define EWS 16
#define NW1B 18
#define NW2 16
#define NB5 5
#define MFMA(a, b, c) __builtin_amdgcn_mfma_f32_32x32x16_bf16(__builtin_bit_cast(bf16x8, a), __builtin_bit_cast(bf16x8, b), c, 0, 0, 0)
#define BAR() asm volatile("s_waitcnt lgkmcnt(0)\n\ts_barrier" ::: "memory")
#define BARX() asm volatile("s_barrier" ::: "memory")
#define LGK() asm volatile("s_waitcnt lgkmcnt(0)" ::: "memory")
#define SB() __builtin_amdgcn_sched_barrier(0)
#define PIN(x) asm volatile("" : "+v"(x))
#define EX2(x) __builtin_amdgcn_exp2f(x)
#define RLX_AGENT __ATOMIC_RELAXED, __HIP_MEMORY_SCOPE_AGENT

#define STAMP(i) do {} while (0)

template <int I> struct IC { static constexpr int value = I; };
template <int I, int E, class F> DI void static_for(F& f) { if constexpr (I < E) { f(IC<I>{}); static_for<I + 1, E>(f); } }
template <int N> DI void waitv() { asm volatile("s_waitcnt vmcnt(%0)" ::"n"(N) : "memory"); }
DI void dma(rsrc_t rs, unsigned voff, unsigned soff, unsigned lds) {
    asm volatile("s_mov_b32 m0, %0\n\ts_nop 0\n\tbuffer_load_dwordx4 %1, %2, %3 offen lds" ::"s"(lds), "v"(voff), "s"(rs), "s"(soff) : "memory");
}
DI void dma_sc1(rsrc_t rs, unsigned voff, unsigned soff, unsigned lds) {
    asm volatile("s_mov_b32 m0, %0\n\ts_nop 0\n\tbuffer_load_dwordx4 %1, %2, %3 offen sc1 lds" ::"s"(lds), "v"(voff), "s"(rs), "s"(soff) : "memory");
}
DI u32x4 ld16(rsrc_t rs, unsigned voff, unsigned soff) { return __builtin_amdgcn_raw_buffer_load_b128(rs, voff, soff, 16); }
DI void st16(rsrc_t rs, u32x4 v, unsigned voff, unsigned soff) { __builtin_amdgcn_raw_buffer_store_b128(v, rs, voff + soff, 0, 16); }
DI void st8(rsrc_t rs, u32x2 v, unsigned voff, unsigned soff) { __builtin_amdgcn_raw_buffer_store_b64(v, rs, voff, soff, 16); }
DI u32x4 lds16(unsigned a) { return *(const LAS u32x4*)(uintptr_t)a; }
DI f32x4 lds16f(unsigned a) { return *(const LAS f32x4*)(uintptr_t)a; }
DI u32x2 ldstr(unsigned a) { return __builtin_bit_cast(u32x2, __builtin_amdgcn_ds_read_tr16_b64_v4i16((LAS s16x4*)(uintptr_t)a)); }
template <int D2> DI u32x4 wfrag(unsigned a) { u32x2 lo = ldstr(a), hi = ldstr(a + D2); return u32x4{lo[0], lo[1], hi[0], hi[1]}; }
DI unsigned pk(float lo, float hi) { return __builtin_bit_cast(unsigned, __builtin_convertvector(f32x2{lo, hi}, bf16x2)); }
DI float bflo(unsigned u) { return __uint_as_float(u << 16); }
DI float bfhi(unsigned u) { return __uint_as_float(u & 0xffff0000u); }
DI float sum32(float v) {
    auto r = __builtin_amdgcn_permlane32_swap(__float_as_uint(v), __float_as_uint(v), false, false);
    return __uint_as_float(r[0]) + __uint_as_float(r[1]);
}
DI float max32(float v) {
    auto r = __builtin_amdgcn_permlane32_swap(__float_as_uint(v), __float_as_uint(v), false, false);
    return fmaxf(__uint_as_float(r[0]), __uint_as_float(r[1]));
}
DI int fresh_lane() { int l; asm volatile("v_mbcnt_lo_u32_b32 %0, -1, 0\n\tv_mbcnt_hi_u32_b32 %0, -1, %0" : "=v"(l)); return l; }
DI float add1(float a, float b) { float d; asm("v_add_f32 %0, %1, %2" : "=v"(d) : "v"(a), "v"(b)); return d; }
DI float mul1(float a, float b) { float d; asm("v_mul_f32 %0, %1, %2" : "=v"(d) : "v"(a), "v"(b)); return d; }
DI float fma1(float a, float b, float c) { float d; asm("v_fma_f32 %0, %1, %2, %3" : "=v"(d) : "v"(a), "v"(b), "v"(c)); return d; }
DI u32x2 silu4(float a0, float a1, float a2, float a3, f32x4 c, float nm, float ce) {
    unsigned o0, o1; float z0, z1, z2, z3, t0, t1, t2, t3;
    asm("v_fma_f32 %2, %14, %10, %16\n\tv_fma_f32 %3, %14, %11, %17\n\tv_fma_f32 %4, %14, %12, %18\n\tv_fma_f32 %5, %14, %13, %19\n\t"
        "v_mul_f32 %6, %15, %2\n\tv_mul_f32 %7, %15, %3\n\tv_mul_f32 %8, %15, %4\n\tv_mul_f32 %9, %15, %5\n\t"
        "v_exp_f32 %6, %6\n\tv_exp_f32 %7, %7\n\tv_exp_f32 %8, %8\n\tv_exp_f32 %9, %9\n\t"
        "v_add_f32 %6, 1.0, %6\n\tv_add_f32 %7, 1.0, %7\n\tv_add_f32 %8, 1.0, %8\n\tv_add_f32 %9, 1.0, %9\n\t"
        "v_rcp_f32 %6, %6\n\tv_rcp_f32 %7, %7\n\tv_rcp_f32 %8, %8\n\tv_rcp_f32 %9, %9\n\t"
        "v_mul_f32 %2, %2, %6\n\tv_mul_f32 %3, %3, %7\n\tv_mul_f32 %4, %4, %8\n\tv_mul_f32 %5, %5, %9\n\t"
        "v_cvt_pk_bf16_f32 %0, %2, %3\n\tv_cvt_pk_bf16_f32 %1, %4, %5"
        : "=v"(o0), "=v"(o1), "=&v"(z0), "=&v"(z1), "=&v"(z2), "=&v"(z3), "=&v"(t0), "=&v"(t1), "=&v"(t2), "=&v"(t3)
        : "v"(c[0]), "v"(c[1]), "v"(c[2]), "v"(c[3]), "v"(nm), "v"(ce), "v"(a0), "v"(a1), "v"(a2), "v"(a3));
    return u32x2{o0, o1};
}
DI float dot2(unsigned a, unsigned b, float c) { return __builtin_amdgcn_fdot2_f32_bf16(__builtin_bit_cast(bf16x2, a), __builtin_bit_cast(bf16x2, b), c, false); }

DI void wait_flags(char* ws, unsigned off, u64 epoch) {
    const GAS u64* p = (const GAS u64*)(ws + off);
    for (unsigned sp = 0; sp < SPIN_CAP; ++sp) {
        const u64 v = __hip_atomic_load(p, RLX_AGENT);
        if (__all(v == epoch)) break;
    }
    asm volatile("" ::: "memory");
}
DI void wait_flags2(char* ws, unsigned off0, unsigned off1, u64 epoch) {
    const GAS u64* p0 = (const GAS u64*)(ws + off0); const GAS u64* p1 = (const GAS u64*)(ws + off1);
    for (unsigned sp = 0; sp < SPIN_CAP; ++sp) {
        const u64 v0 = __hip_atomic_load(p0, RLX_AGENT), v1 = __hip_atomic_load(p1, RLX_AGENT);
        if (__all(v0 == epoch && v1 == epoch)) break;
    }
    asm volatile("" ::: "memory");
}
DI void set_flag(char* ws, unsigned off, u64 epoch) { __hip_atomic_store((GAS u64*)(ws + off), epoch, RLX_AGENT); }

__global__ void __launch_bounds__(512, 2)
block_kernel(const void* __restrict__ gx, const void* __restrict__ gwqkv, const void* __restrict__ gwo, const void* __restrict__ gw1,
             const void* __restrict__ gw2, void* __restrict__ gout, char* __restrict__ ws) {
    extern __shared__ __attribute__((aligned(16))) char smem[];
    const int tid = threadIdx.x;
    const int w = __builtin_amdgcn_readfirstlane(tid >> 6);
    const int b = blockIdx.x, xcd = b & 7, bi = b >> 3;
    const rsrc_t rX = __builtin_amdgcn_make_buffer_rsrc((void*)gx, 0, SEQ * DM * 2, 0x00020000);
    const rsrc_t rWqkv = __builtin_amdgcn_make_buffer_rsrc((void*)gwqkv, 0, DM * 3 * DM * 2, 0x00020000);
    const rsrc_t rW1 = __builtin_amdgcn_make_buffer_rsrc((void*)gw1, 0, WSZ(DM * FF * 2), 0x00020000);
    const rsrc_t rW2 = __builtin_amdgcn_make_buffer_rsrc((void*)gw2, 0, WSZ(FF * DM * 2), 0x00020000);
    const rsrc_t rOut = __builtin_amdgcn_make_buffer_rsrc(gout, 0, SEQ * DM * 2, 0x00020000);
    const rsrc_t rWs = __builtin_amdgcn_make_buffer_rsrc(ws, 0, WS_END, 0x00020000);
    const int tb1 = 4 * (xcd >> 1) + bi / 7, c1 = 7 * (xcd & 1) + bi % 7;
    const int h2 = 2 * xcd + (bi & 1), qb = bi >> 1;
    const int tb3 = 2 * xcd + (bi >> 4), j3 = bi & 15, mh = j3 & 1, cb = j3 >> 1;
    const bool act3 = j3 < 14;
    const int g4 = (bi >> 4) * 2 + mh, r4 = 7 * g4;
#define R4(s) ((s) + r4 >= 28 ? (s) + r4 - 28 : (s) + r4)
    u32x4 fa[16];
    STAMP(31); STAMP(0);
    const u64 epoch = *(const __attribute__((address_space(4))) u64*)(ws + WS_EPOCH) + 1;
    {
    const int lane = tid & 63, r = lane & 31, hh = lane >> 5;
    const unsigned trn = (unsigned)((8 * hh + ((lane & 15) >> 2)) * 64 + ((lane >> 4) & 1) * 32 + (lane & 3) * 8);
    const unsigned trp = (unsigned)((4 * hh + ((lane & 15) >> 2)) * 64 + ((lane >> 4) & 1) * 32 + (lane & 3) * 8);
    const unsigned v1 = (lane >> 2) * 3584 + (lane & 3) * 16;
    const unsigned v2 = (lane >> 2) * 896 + (lane & 3) * 16;
    const unsigned lo = lane * 16;
    if (tid == 0) *(LAS u32x4*)(uintptr_t)L_BAD = u32x4{0u, 0u, 0u, 0u};

    if (w == 0) dma(rX, lane < 56 ? lane * 128 : OOB, (unsigned)b * 7168, L_M + 4096);
    if (w == 1) dma(rWqkv, lane < 37 ? lane * 128 : OOB, (unsigned)b * 4704, L_M + 4096);
    if (w == 3) dma_sc1(rWs, lane < 16 ? lane * 2097152 : lane == 16 ? WS_F1 + c1 * 256 : lane == 17 ? WS_F2 + qb * 256 : lane == 18 ? WS_F3 + (tb3 * 2 + mh) * 256 : lane == 19 ? WS_F4 + (tb3 * 2 + mh) * 256 : OOB, 0, L_M + 4096);
    if (w == 7) dma(rOut, lane < 7 ? lane * 262144 : OOB, 0, L_M + 4096);
    if (w == 2) {
        u64 pc; asm volatile("s_getpc_b64 %0" : "=s"(pc));
        dma(__builtin_amdgcn_make_buffer_rsrc((void*)(pc & ~127ull), 0, CODE_PF, 0x00020000), lane < 10 ? lane * 128 : OOB, (unsigned)bi * 1280, L_M + 4096);
    }
    if (bi >= 28) {
        const int id = (bi - 28) * 8 + xcd, u = id * 8 + w;
        const int kind = u < 112 ? 0 : u < 224 ? 1 : 2, v = u - 112 * kind;
        const int pair = kind == 1 ? v >> 4 : v >> 2, seg = kind == 1 ? v & 15 : v & 3;
        const rsrc_t rs = __builtin_amdgcn_make_buffer_rsrc((void*)(kind == 0 ? gw1 : kind == 1 ? gw2 : gwo), 0, u >= 224 ? 0 : kind == 2 ? DM * DM * 2 : DM * FF * 2, 0x00020000);
        const unsigned rowb = kind == 0 ? 16 * 3584 : 16 * 896, nks = kind == 1 ? 112 : 28;
        const unsigned so = (unsigned)(seg * 7) * rowb + pair * 128, ld = w * 14 * KB;
#pragma unroll
        for (int i = 0; i < 7; ++i) { dma(rs, kind == 0 ? v1 : v2, so + i * rowb, ld + 2 * i * KB); dma(rs, kind == 0 ? v1 : v2, so + i * rowb + 64, ld + (2 * i + 1) * KB); }
        const unsigned dst = u >= 224 ? OOB : (kind == 0 ? WS_W1T : kind == 1 ? WS_W2T : WS_WOT) + (2 * pair * nks + seg * 7) * 1024;
        STAMP(10);
        waitv<0>();
        STAMP(11);
        float c[2] = {0.f, 0.f};
#pragma unroll
        for (int i = 0; i < 14; ++i) {
            const u32x4 f = wfrag<512>(ld + trp + i * KB);
            st16(rWs, f, lo + (i >> 1) * 1024, dst + (i & 1) * nks * 1024);
#pragma unroll
            for (int e = 0; e < 4; ++e) c[i & 1] += bflo(f[e]) + bfhi(f[e]);
        }
        STAMP(12);
        c[0] = sum32(c[0]); c[1] = sum32(c[1]);
        *(LAS float*)(uintptr_t)(L_M + w * 256 + lane * 4) = hh ? c[1] : c[0];
        BAR();
        if (id < 14 && w < 2) {
            float t = 0.f;
#pragma unroll
            for (int i = 0; i < 4; ++i) t += *(const LAS float*)(uintptr_t)(L_M + (w * 4 + i) * 256 + lane * 4);
            __builtin_amdgcn_raw_buffer_store_b32(__float_as_uint(t), rWs, lane * 4, WS_CS1 + (unsigned)(id * 2 + w) * 256, 16);
        }
        waitv<0>();
        STAMP(13);
        BAR();
        if (tid == 0) set_flag(ws, WS_FS + id * 8, epoch);
    } else {
#define ROTV 0
      const int rot = ROTV == 0 ? 0 : ROTV == 1 ? (c1 + 2 * tb1) % 7 : ((c1 + tb1) & 1);
      if (w < 4) {
        const int rin = lane >> 3, sl = lane & 7;
        const unsigned ve = rin * 896 + ((sl ^ (rin >> 1)) * 16), vo = rin * 896 + ((sl ^ (4 + (rin >> 1))) * 16);
        const unsigned xs = (unsigned)(tb1 * 128 + w * 32) * 896, xl = L_XR + w * 16 * KB;
#define ROT(c) ((c) + rot >= 7 ? (c) + rot - 7 : (c) + rot)
#define XDMA(c) do { const unsigned o_ = xs + ROT(c) * 128; _Pragma("unroll") for (int g = 0; g < 4; ++g) dma(rX, (g & 1) ? vo : ve, o_ + g * 8 * 896, xl + ((c) & 3) * 4 * KB + g * KB); } while (0)
        XDMA(0); XDMA(1);
        const unsigned xb = xl + (r >> 3) * KB + (r & 7) * 128 + ((hh ^ ((r >> 1) & 7)) * 16);
        u32x4 xf[8];
#define XRD(c) do { _Pragma("unroll") for (int s = 0; s < 4; ++s) xf[4 * ((c) & 1) + s] = lds16((xb ^ (s * 32)) + ((c) & 3) * 4 * KB); } while (0)
        f32x16 ac[3] = {{}, {}, {}};
        unsigned wbA = L_WR + trn, wbB = L_WR + trn + 63 * KB;
        PIN(wbA); PIN(wbB);
        u32x4 fw[3][3];
#define WRD(ks) do { _Pragma("unroll") for (int t = 0; t < 3; ++t) fw[(ks) % 3][t] = (ks) < 21 ? wfrag<256>(wbA + ((ks) * 3 + t) * KB) : wfrag<256>(wbB + (((ks) - 21) * 3 + t) * KB); } while (0)
        float s1 = 0.f, s2 = 0.f;
#define XST(c) do { _Pragma("unroll") for (int q = 0; q < 4; ++q) _Pragma("unroll") for (int e = 0; e < 4; ++e) { s1 = dot2(xf[4 * ((c) & 1) + q][e], 0x3F803F80u, s1); s2 = dot2(xf[4 * ((c) & 1) + q][e], xf[4 * ((c) & 1) + q][e], s2); } PIN(s1); PIN(s2); SB(); } while (0)
        waitv<4>(); XRD(0); XDMA(2); XST(0);
        BARX(); WRD(0); WRD(1); SB();
#pragma unroll
        for (int ks = 0; ks < 28; ++ks) {
            ac[0] = MFMA(fw[ks % 3][0], xf[ks & 7], ac[0]); PIN(ac[0]);
            SB();
            if (ks + 2 < 28) {
                if ((ks + 2) % 4 == 0) {
                    const int c = (ks + 2) / 4;
                    if (c < 6) waitv<4>(); else waitv<0>();
                    XRD(c);
                    if (c + 2 < 7) XDMA(c + 2);
                    BARX();
                }
                WRD(ks + 2);
                if ((ks + 2) % 4 == 1 && ks > 2) XST((ks + 2) / 4);
            }
            SB();
            ac[1] = MFMA(fw[ks % 3][1], xf[ks & 7], ac[1]); PIN(ac[1]); ac[2] = MFMA(fw[ks % 3][2], xf[ks & 7], ac[2]); PIN(ac[2]);
            SB();
        }
        STAMP(12);
        s1 = sum32(s1); s2 = sum32(s2);
        const float mean = s1 * (1.f / DM), rstd = __builtin_amdgcn_rsqf(fmaxf(s2 * (1.f / DM) - mean * mean, 0.f) + 1e-5f);
        BAR();
        STAMP(13);
        f32x4 csr[3][4];
#pragma unroll
        for (int t = 0; t < 3; ++t)
#pragma unroll
            for (int g = 0; g < 4; ++g) csr[t][g] = lds16f(L_CS + t * 128 + (8 * g + 4 * hh) * 4);
        const unsigned kt = (unsigned)(tb1 * 4 + w);
        u32x2 pkd[3][4];
#pragma unroll
        for (int t = 0; t < 3; ++t) {
            const float sc = 3 * c1 + t < 14 ? rstd * (0.18898223650461362f * 1.4426950408889634f) : rstd, ms = -mean * sc;
#pragma unroll
            for (int g = 0; g < 4; ++g) {
                float y[4];
#pragma unroll
                for (int e = 0; e < 4; ++e) y[e] = fmaf(ac[t][4 * g + e], sc, ms * csr[t][g][e]);
                pkd[t][g] = u32x2{pk(y[0], y[1]), pk(y[2], y[3])};
            }
            if (3 * c1 + t >= 28) {
#pragma unroll
                for (int g = 0; g < 4; ++g) *(LAS u32x2*)(uintptr_t)(L_SCR + (w * 3 + t) * 2 * KB + r * 64 + (2 * g + hh) * 8) = pkd[t][g];
                asm volatile("" ::: "memory");
                const u32x4 f0 = wfrag<512>(L_SCR + (w * 3 + t) * 2 * KB + trp), f1 = wfrag<512>(L_SCR + (w * 3 + t) * 2 * KB + trp + 1024);
                pkd[t][0] = u32x2{f0[0], f0[1]}; pkd[t][1] = u32x2{f0[2], f0[3]}; pkd[t][2] = u32x2{f1[0], f1[1]}; pkd[t][3] = u32x2{f1[2], f1[3]};
            } else {
                const int nt = 3 * c1 + t;
                const unsigned base = WS_Q + kt * 28672 + (nt < 14 ? (unsigned)nt * 2048 : WS_K - WS_Q + (unsigned)(nt - 14) * 2048);
#pragma unroll
                for (int g = 0; g < 4; g += 2) {
                    const auto x0 = __builtin_amdgcn_permlane32_swap(pkd[t][g][0], pkd[t][g + 1][0], false, false), x1 = __builtin_amdgcn_permlane32_swap(pkd[t][g][1], pkd[t][g + 1][1], false, false);
                    st16(rWs, u32x4{x0[0], x1[0], x0[1], x1[1]}, lo + g * 512, base);
                }
            }
        }
#pragma unroll
        for (int t = 0; t < 3; ++t) {
            if (3 * c1 + t >= 28) {
                const unsigned off = (unsigned)(hh * 7168 + ((3 * c1 + t - 28) * 32 + r) * 16);
                st16(rWs, u32x4{pkd[t][0][0], pkd[t][0][1], pkd[t][1][0], pkd[t][1][1]}, off, WS_V + kt * 28672);
                st16(rWs, u32x4{pkd[t][2][0], pkd[t][2][1], pkd[t][3][0], pkd[t][3][1]}, off + 14336, WS_V + kt * 28672);
            }
        }
        STAMP(14);
        waitv<0>();
        if (lane == 0 && __hip_atomic_fetch_add((LAS unsigned*)(uintptr_t)L_CNT, 1u, __ATOMIC_RELAXED, __HIP_MEMORY_SCOPE_WORKGROUP) == 3u) set_flag(ws, WS_F1 + c1 * 256 + tb1 * 8, epoch);
      } else {
        const int j = w - 4;
        const unsigned vw = (lane >> 2) * 2688 + (lane & 3) * 16;
        const unsigned so = (unsigned)(j * 16 * 2688 + c1 * 192), ld = L_WR + j * 3 * KB;
#define WDMA(st) do { const unsigned o_ = so + ROT(st) * 64 * 2688; dma(rWqkv, vw, o_, ld + (st) * 12 * KB); dma(rWqkv, vw, o_ + 64, ld + (st) * 12 * KB + KB); dma(rWqkv, vw, o_ + 128, ld + (st) * 12 * KB + 2 * KB); } while (0)
        WDMA(0); WDMA(1);
        const u32x4 ones = {0x3F803F80u, 0x3F803F80u, 0x3F803F80u, 0x3F803F80u};
        f32x16 cacc = {};
        unsigned wb = L_WR + trn + (j < 3 ? j : 0) * KB;
#pragma unroll
        for (int st = 0; st < 7; ++st) {
            if (st < 5) { waitv<3>(); WDMA(st + 2); } else if (st == 5) waitv<4>(); else waitv<1>();
            if (st == 4) dma(__builtin_amdgcn_make_buffer_rsrc((void*)gwo, 0, DM * DM * 2, 0x00020000), lane < 14 ? lane * 128 : OOB, j == 3 ? (unsigned)(bi * 8 + xcd) * 1792 : OOB, L_M + 4096);
            BARX();
            if (j < 3) {
                u32x4 f[4];
#pragma unroll
                for (int q = 0; q < 4; ++q) f[q] = wfrag<256>(wb + q * 3 * KB);
#pragma unroll
                for (int q = 0; q < 4; ++q) cacc = MFMA(ones, f[q], cacc);
                wb += 12 * KB; PIN(wb); PIN(cacc);
            }
            if (st == 0) STAMP(10); if (st == 3) STAMP(11);
        }
        if (j < 3 && hh == 0) *(LAS float*)(uintptr_t)(L_CS + j * 128 + r * 4) = cacc[0];
        BAR();
        while (*(const volatile LAS unsigned*)(uintptr_t)L_CNT < 4u) __builtin_amdgcn_s_sleep(1);
      }
    }
    STAMP(1); STAMP(25);

    {
        const int qh = w & 1, kq = w >> 1;
        __builtin_amdgcn_s_setprio(3);
        const unsigned ch = (unsigned)((28 * h2) >> 3) * 512;
        const unsigned qso = WS_Q + (unsigned)(qb * 4 + qh * 2) * 28672 + ch;
        const rsrc_t rK = __builtin_amdgcn_make_buffer_rsrc(ws + WS_K + (unsigned)kq * 16 * 28672 + ch, 0, 15 * 28672 + 2048, 0x00020000);
        const rsrc_t rV = __builtin_amdgcn_make_buffer_rsrc(ws + WS_V + (unsigned)kq * 16 * 28672 + (unsigned)h2 * 448, 0, 16 * 28672 - h2 * 448, 0x00020000);
        unsigned vo0 = (unsigned)(hh * 7168 + r * 16), vo1 = vo0 + 14336;
        const unsigned vpad = r == DH ? 0x3F803F80u : 0u;
        unsigned qm0 = ((h2 & 1) && !hh) ? 0u : ~0u, qm1 = (!(h2 & 1) && hh) ? 0u : ~0u;
        u32x4 kf[4][2], vf[4][2], pa[2], pb[2], qa0, qa1, qb0, qb1;
        f32x16 oa = {}, ob = {}, sa, sb = {}, ca, cbv;
#pragma unroll
        for (int i = 0; i < 4; ++i) { vf[i][0] = vf[i][1] = u32x4{vpad, vpad, vpad, vpad}; PIN(vf[i][0]); PIN(vf[i][1]); }
        pb[0] = pb[1] = u32x4{0u, 0u, 0u, 0u};
        PIN(oa); PIN(ob); PIN(sb); PIN(pb[0]); PIN(pb[1]); PIN(vo0); PIN(vo1); PIN(qm0); PIN(qm1);
        SB();
        if (w == 0) {
            const int c0 = (28 * h2) & ~7;
            const int col = lane < 32 ? 448 + c0 + 31 * ((lane >> 4) & 1) : 896 + 28 * h2 + 27 * ((lane >> 4) & 1);
            wait_flags2(ws, WS_F1 + (unsigned)(col / 96) * 256 + (lane & 15) * 8, WS_F1 + (unsigned)((c0 + 31 * (lane & 1)) / 96) * 256 + qb * 8, epoch);
            *(volatile LAS unsigned*)(uintptr_t)L_GO = 1u;
        } else {
            if (w >= 4 && act3) {
                const rsrc_t rWo = __builtin_amdgcn_make_buffer_rsrc((void*)gwo, 0, DM * DM * 2, 0x00020000);
                __builtin_amdgcn_s_sleep(EWS);
#pragma unroll
                for (int i = 0; i < 8; ++i) { const int id = 4 * i + w - 4; const unsigned so = (unsigned)(((id >> 1) * 28 + (id & 1) * 16) * 896 + cb * 128); dma(rWo, v2, so, L_A + id * 2 * KB); dma(rWo, v2, so + 64, L_A + id * 2 * KB + KB); }
            }
            while (*(const volatile LAS unsigned*)(uintptr_t)L_GO == 0u) __builtin_amdgcn_s_sleep(1);
            asm volatile("" ::: "memory");
        }
        STAMP(2); STAMP(26);
        asm volatile("buffer_load_dwordx4 %0, %6, %7, %8 offen sc1\n\tbuffer_load_dwordx4 %1, %6, %7, %8 offen offset:1024 sc1\n\t"
                     "buffer_load_dwordx4 %4, %6, %7, %10 offen sc1\n\tbuffer_load_dwordx4 %5, %6, %7, %10 offen offset:1024 sc1\n\t"
                     "buffer_load_dwordx4 %2, %6, %7, %9 offen sc1\n\tbuffer_load_dwordx4 %3, %6, %7, %9 offen offset:1024 sc1"
                     : "=&v"(qa0), "=&v"(qa1), "=&v"(qb0), "=&v"(qb1), "=&v"(pa[0]), "=&v"(pa[1]) : "v"(lo), "s"(rWs), "s"(qso), "s"(qso + 28672), "s"(WS_K + ch) : "memory");
#define KVLD(KC, K0, K1, V0, V1, SK, SV) asm volatile("buffer_load_dwordx4 %0, %4, %7, %9 offen sc1\n\tbuffer_load_dwordx4 %1, %4, %7, %9 offen offset:1024 sc1\n\t" \
                "s_mov_b64 exec, %11\n\tbuffer_load_dwordx4 %2, %5, %8, %10 offen sc1\n\tbuffer_load_dwordx4 %3, %6, %8, %10 offen sc1\n\ts_mov_b64 exec, -1" \
                : KC(K0), KC(K1), "+v"(V0), "+v"(V1) : "v"(lo), "v"(vo0), "v"(vo1), "s"(rK), "s"(rV), "s"(SK), "s"(SV), "s"(0x0fffffff0fffffffull) : "memory")
#pragma unroll
        for (int i = 0; i < 4; ++i) KVLD("=&v", kf[i][0], kf[i][1], vf[(i + 3) & 3][0], vf[(i + 3) & 3][1], (unsigned)(i * 28672), i ? (unsigned)((i - 1) * 28672) : OOB);
        asm volatile("s_waitcnt vmcnt(16)" : "+v"(qa0), "+v"(qa1), "+v"(qb0), "+v"(qb1), "+v"(pa[0]), "+v"(pa[1]) :: "memory");
        qa0[0] &= qm0; qa0[1] &= qm0; qb0[0] &= qm0; qb0[1] &= qm0; qa1[2] &= qm1; qa1[3] &= qm1; qb1[2] &= qm1; qb1[3] &= qm1;
        {
            const f32x16 z = {};
            ca = MFMA(pa[0], qa0, z); cbv = MFMA(pa[0], qb0, z); ca = MFMA(pa[1], qa1, ca); cbv = MFMA(pa[1], qb1, cbv);
            float ma = ca[0], mb = cbv[0];
#pragma unroll
            for (int i = 1; i < 16; ++i) { ma = fmaxf(ma, ca[i]); mb = fmaxf(mb, cbv[i]); }
            ma = -max32(ma); mb = -max32(mb);
#pragma unroll
            for (int i = 0; i < 16; ++i) { ca[i] = ma; cbv[i] = mb; }
        }
        PIN(ca); PIN(cbv);
        asm volatile("s_waitcnt vmcnt(12)" : "+v"(kf[0][0]), "+v"(kf[0][1]), "+v"(vf[3][0]), "+v"(vf[3][1]) :: "memory");
        sa = MFMA(kf[0][0], qa0, ca); sa = MFMA(kf[0][1], qa1, sa);
        PIN(sa); STAMP(15);
        SB();
#define GRP(MF, S, i, T, j, P) do { MF; SB(); \
        S[4 * (i)] = EX2(S[4 * (i)]); S[4 * (i) + 1] = EX2(S[4 * (i) + 1]); S[4 * (i) + 2] = EX2(S[4 * (i) + 2]); S[4 * (i) + 3] = EX2(S[4 * (i) + 3]); SB(); \
        P[(j) >> 1][((j) & 1) * 2] = pk(T[4 * (j)], T[4 * (j) + 1]); P[(j) >> 1][((j) & 1) * 2 + 1] = pk(T[4 * (j) + 2], T[4 * (j) + 3]); PIN(P[(j) >> 1]); PIN(S); SB(); } while (0)
#pragma unroll 1
        for (int tq = 0; tq < 4; ++tq) {
            const unsigned sk = (unsigned)tq * 4 * 28672;
            if (tq == 1) __builtin_amdgcn_s_setprio(2); if (tq == 2) __builtin_amdgcn_s_setprio(1); if (tq == 3) __builtin_amdgcn_s_setprio(0);
#pragma unroll
            for (int u = 0; u < 4; ++u) {
                GRP(ob = MFMA(vf[(u + 3) & 3][0], pb[0], ob), sa, 0, sb, 3, pb);
                GRP(sb = MFMA(kf[u][0], qb0, cbv), sa, 1, sa, 0, pa);
                GRP(sb = MFMA(kf[u][1], qb1, sb), sa, 2, sa, 1, pa);
                GRP(ob = MFMA(vf[(u + 3) & 3][1], pb[1], ob), sa, 3, sa, 2, pa);
                KVLD("+v", kf[u][0], kf[u][1], vf[(u + 3) & 3][0], vf[(u + 3) & 3][1], sk + (u + 4) * 28672, sk + (u + 3) * 28672);
                asm volatile("s_waitcnt vmcnt(12)" : "+v"(kf[(u + 1) & 3][0]), "+v"(kf[(u + 1) & 3][1]), "+v"(vf[u][0]), "+v"(vf[u][1]) :: "memory");
                SB();
                GRP(oa = MFMA(vf[u][0], pa[0], oa), sb, 0, sa, 3, pa);
                GRP(sa = MFMA(kf[(u + 1) & 3][0], qa0, ca), sb, 1, sb, 0, pb);
                GRP(sa = MFMA(kf[(u + 1) & 3][1], qa1, sa), sb, 2, sb, 1, pb);
                GRP(oa = MFMA(vf[u][1], pa[1], oa), sb, 3, sb, 2, pb);
            }
        }
        asm volatile("s_waitcnt vmcnt(0)" : "+v"(kf[0][0]), "+v"(kf[0][1]), "+v"(kf[1][0]), "+v"(kf[1][1]), "+v"(kf[2][0]), "+v"(kf[2][1]), "+v"(kf[3][0]), "+v"(kf[3][1]),
                     "+v"(vf[0][0]), "+v"(vf[0][1]), "+v"(vf[1][0]), "+v"(vf[1][1]), "+v"(vf[2][0]), "+v"(vf[2][1]) :: "memory");
        pb[1][2] = pk(sb[12], sb[13]); pb[1][3] = pk(sb[14], sb[15]);
        ob = MFMA(vf[3][0], pb[0], ob); ob = MFMA(vf[3][1], pb[1], ob);
        PIN(ob); STAMP(16);
        const int lane = fresh_lane(), r = lane & 31, hh = lane >> 5;
        if (w == 0) wait_flags(ws, WS_FS + (unsigned)r * 8, epoch);
        const unsigned lo = lane * 16;
        unsigned cw = L_CMB + (unsigned)(qh * 16 + kq) * 2048 + lo;
        PIN(cw);
#pragma unroll 1
        for (int pass = 0; pass < 2; ++pass) {
#pragma unroll
            for (int p = 0; p < 4; ++p) {
                const f32x16& o = (p >> 1) ? ob : oa;
                *(LAS f32x4*)(uintptr_t)(cw + p * 8192) = f32x4{o[8 * (p & 1)], o[8 * (p & 1) + 1], o[8 * (p & 1) + 2], o[8 * (p & 1) + 3]};
                *(LAS f32x4*)(uintptr_t)(cw + p * 8192 + 1024) = f32x4{o[8 * (p & 1) + 4], o[8 * (p & 1) + 5], o[8 * (p & 1) + 6], o[8 * (p & 1) + 7]};
            }
            unsigned so0 = WS_O + (unsigned)(((qb * 4 + qh * 2 + (kq >> 1)) * 16 + h2) * 2 + (kq & 1)) * 1024 + lo;
            unsigned cr = L_CMB + (unsigned)(qh * 4 + kq) * 8192 + lo, cl = L_CMB + (unsigned)(qh * 4 + (kq | 1)) * 8192 + 1024 + (32 + r) * 16;
            PIN(so0); PIN(cr); PIN(cl);
            BAR();
            STAMP(22);
            f32x4 y0 = lds16f(cr), y1 = lds16f(cr + 1024);
            float l = *(const LAS float*)(uintptr_t)cl;
#pragma unroll
            for (int i = 1; i < 4; ++i) { y0 += lds16f(cr + i * 2048); y1 += lds16f(cr + i * 2048 + 1024); l += *(const LAS float*)(uintptr_t)(cl + i * 2048); }
            const float il = __builtin_amdgcn_rcpf(l);
            const float i1 = ((kq & 1) && hh) ? 0.f : il;
            const auto x0 = __builtin_amdgcn_permlane32_swap(pk(y0[0] * il, y0[1] * il), pk(y1[0] * i1, y1[1] * i1), false, false);
            const auto x1 = __builtin_amdgcn_permlane32_swap(pk(y0[2] * il, y0[3] * il), pk(y1[2] * i1, y1[3] * i1), false, false);
            st16(rWs, u32x4{x0[0], x1[0], x0[1], x1[1]}, so0, 0);
            const float t = ((fabsf(y0[0]) + fabsf(y0[1])) + (fabsf(y0[2]) + fabsf(y0[3]))) + ((fabsf(y1[0]) + fabsf(y1[1])) + (fabsf(y1[2]) + fabsf(y1[3]))) + l;
            if (__any(!(t < 1.0e36f))) *(LAS unsigned*)(uintptr_t)L_BAD = 1;
            if (act3) {
                const unsigned fb = L_A + (unsigned)((8 * hh + ((lane & 15) >> 2)) * 64 + ((lane >> 4) & 1) * 32 + (lane & 3) * 8) + kq * 16 * KB;
#pragma unroll
                for (int i = 0; i < 16; ++i) fa[i] = wfrag<256>(fb + i * KB);
            }
            waitv<0>();
            BAR();
            if (pass == 1 || !__builtin_amdgcn_readfirstlane(*(const volatile LAS unsigned*)(uintptr_t)L_BAD)) break;
            const f32x16 z = {};
            float ma = -INFINITY, mb = -INFINITY;
#pragma unroll 1
            for (int t = 0; t < 16; ++t) {
                const u32x4 k0 = ld16(rK, lo, t * 28672), k1 = ld16(rK, lo + 1024, t * 28672);
                f32x16 ta = MFMA(k0, qa0, z), tb = MFMA(k0, qb0, z); ta = MFMA(k1, qa1, ta); tb = MFMA(k1, qb1, tb);
#pragma unroll
                for (int i = 0; i < 16; ++i) { ma = fmaxf(ma, ta[i]); mb = fmaxf(mb, tb[i]); }
            }
            ma = max32(ma); mb = max32(mb);
            *(LAS float*)(uintptr_t)(L_M + 2048 + ((kq * 2 + qh) * 2 + hh) * 128 + r * 4) = hh ? mb : ma;
            BAR();
#pragma unroll
            for (int i = 0; i < 4; ++i) { ma = fmaxf(ma, *(const LAS float*)(uintptr_t)(L_M + 2048 + ((i * 2 + qh) * 2) * 128 + r * 4)); mb = fmaxf(mb, *(const LAS float*)(uintptr_t)(L_M + 2048 + ((i * 2 + qh) * 2 + 1) * 128 + r * 4)); }
            oa = z; ob = z;
            const unsigned vp = r == DH ? 0x3F803F80u : 0u, vm = r < DH ? ~0u : 0u, vo = (unsigned)(hh * 7168 + (r < DH ? r : 0) * 16);
#pragma unroll 1
            for (int t = 0; t < 16; ++t) {
                const u32x4 k0 = ld16(rK, lo, t * 28672), k1 = ld16(rK, lo + 1024, t * 28672);
                const u32x4 v0 = (ld16(rV, vo, t * 28672) & vm) | vp, v1 = (ld16(rV, vo + 14336, t * 28672) & vm) | vp;
                f32x16 ta = MFMA(k0, qa0, z), tb = MFMA(k0, qb0, z); ta = MFMA(k1, qa1, ta); tb = MFMA(k1, qb1, tb);
                u32x4 p0, p1, p2, p3;
#pragma unroll
                for (int i = 0; i < 4; ++i) {
                    p0[i] = pk(EX2(ta[2 * i] - ma), EX2(ta[2 * i + 1] - ma)); p1[i] = pk(EX2(ta[8 + 2 * i] - ma), EX2(ta[9 + 2 * i] - ma));
                    p2[i] = pk(EX2(tb[2 * i] - mb), EX2(tb[2 * i + 1] - mb)); p3[i] = pk(EX2(tb[8 + 2 * i] - mb), EX2(tb[9 + 2 * i] - mb));
                }
                oa = MFMA(v0, p0, oa); oa = MFMA(v1, p1, oa); ob = MFMA(v0, p2, ob); ob = MFMA(v1, p3, ob);
            }
        }
    }
    }
    const int lane = fresh_lane(), r = lane & 31, hh = lane >> 5;
    const unsigned lo = lane * 16;
    if (tid == 0) set_flag(ws, WS_F2 + qb * 256 + h2 * 8, epoch);
    if (!act3) {
        if (b == 14 * 8 && w == 0) {
#pragma unroll 1
            for (int i = 0; i < 4; ++i) wait_flags(ws, WS_F2 + (unsigned)((i * 4 + (lane >> 4)) * 256 + (lane & 15) * 8), epoch);
            if (lane == 0) set_flag(ws, WS_EPOCH, epoch);
        }
        return;
    }

    const int mt = w & 1, kq = w >> 1;
    const unsigned tt0 = (unsigned)(tb3 * 4 + mh * 2), grp = (unsigned)(tb3 * 2 + mh);
    const unsigned xo = ((tt0 + mt) * 32 + r) * 896 + (cb * 64 + 16 * kq + 4 * hh) * 2;
    const unsigned w1b = WS_W1T + (unsigned)(cb * 8 + w) * 28 * 1024;
    u32x4 fr[28];
    u32x4 hres;
    f32x4 cs[4];
    {
        STAMP(3); STAMP(27);
        const u32x2 xr0 = __builtin_amdgcn_raw_buffer_load_b64(rX, xo, 0, 0), xr1 = __builtin_amdgcn_raw_buffer_load_b64(rX, xo + 16, 0, 0);
#pragma unroll
        for (int g = 0; g < 4; ++g) cs[g] = __builtin_bit_cast(f32x4, ld16(rWs, (8 * g + 4 * hh) * 4, WS_CS1 + (unsigned)(cb * 256 + w * 32) * 4));
#pragma unroll
        for (int i = 0; i < NW1A; ++i) fr[i] = ld16(rWs, lo, w1b + R4(i) * 1024);
        if (w == 0) wait_flags(ws, WS_F2 + tb3 * 256 + (lane & 15) * 8, epoch);
        BARX();
        STAMP(4); STAMP(28);
        u32x4 bf[8];
#pragma unroll
        for (int i = 0; i < 8; ++i) bf[i] = ld16(rWs, lo + (i & 3) * 1024, WS_O + ((tt0 + mt) * 32 + 8 * kq + (i & 4)) * 1024);
        SB();
        f32x16 a0 = {}, a1 = {};
#pragma unroll
        for (int i = 0; i < 8; ++i) { a0 = MFMA(fa[2 * i], bf[i], a0); a1 = MFMA(fa[2 * i + 1], bf[i], a1); }
        PIN(a0); PIN(a1);
        SB();
#pragma unroll
        for (int i = NW1A; i < NW1B; ++i) fr[i] = ld16(rWs, lo, w1b + R4(i) * 1024);
        SB();
        STAMP(23);
        {
            const unsigned dst = L_RED + (unsigned)(mt * 16 + kq) * 2048 + lo;
#pragma unroll
            for (int p = 0; p < 4; ++p) {
                const f32x16& o = (p >> 1) ? a1 : a0;
                *(LAS f32x4*)(uintptr_t)(dst + p * 8192) = f32x4{o[8 * (p & 1)], o[8 * (p & 1) + 1], o[8 * (p & 1) + 2], o[8 * (p & 1) + 3]};
                *(LAS f32x4*)(uintptr_t)(dst + p * 8192 + 1024) = f32x4{o[8 * (p & 1) + 4], o[8 * (p & 1) + 5], o[8 * (p & 1) + 6], o[8 * (p & 1) + 7]};
            }
        }
        float o8[8] = {bflo(xr0[0]), bfhi(xr0[0]), bflo(xr0[1]), bfhi(xr0[1]), bflo(xr1[0]), bfhi(xr1[0]), bflo(xr1[1]), bfhi(xr1[1])};
        unsigned src = L_RED + (unsigned)(mt * 4 + kq) * 8192 + lo;
#pragma unroll
        for (int i = 0; i < 8; ++i) PIN(o8[i]);
        PIN(src);
        BAR();
        STAMP(24);
#pragma unroll
        for (int i = 0; i < 4; ++i) {
            const f32x4 a = lds16f(src + i * 2048), c = lds16f(src + i * 2048 + 1024);
#pragma unroll
            for (int e = 0; e < 4; ++e) { o8[e] += a[e]; o8[4 + e] += c[e]; }
        }
        hres = u32x4{pk(o8[0], o8[1]), pk(o8[2], o8[3]), pk(o8[4], o8[5]), pk(o8[6], o8[7])};
        st16(rWs, hres, lo, WS_H + ((tt0 + mt) * 28 + cb * 4 + kq) * 1024);
        float s1 = 0.f, s2 = 0.f;
#pragma unroll
        for (int e = 0; e < 4; ++e) { const float a = bflo(hres[e]), c = bfhi(hres[e]); s1 += a + c; s2 = fmaf(a, a, fmaf(c, c, s2)); }
        s1 = sum32(s1); s2 = sum32(s2);
        st8(rWs, u32x2{__float_as_uint(s1), __float_as_uint(s2)}, hh ? OOB : r * 8, WS_ST + ((tt0 + mt) * 28 + cb * 4 + kq) * 256);
        waitv<0>();
    }
    STAMP(5);
    float rstd2;
    u32x4 f2[28];
    const int r5 = 2 * ((cb + 2 * g4) % 7);
    const unsigned w2b = WS_W2T + (unsigned)(cb * 2 * 112 + 14 * w) * 1024;
#define R5(t) ((t) + r5 >= 14 ? (t) + r5 - 14 : (t) + r5)
    {
        BARX();
        if (tid == 0) set_flag(ws, WS_F3 + grp * 256 + cb * 8, epoch);
        SB();
#pragma unroll
        for (int i = NW1B; i < 28; ++i) fr[i] = ld16(rWs, lo, w1b + R4(i) * 1024);
#pragma unroll
        for (int i = 0; i < 28; ++i) PIN(fr[i]);
        u32x2 stv[7];
        if (w < 4) {
            if (w == 0) {
                wait_flags(ws, WS_F3 + grp * 256 + (unsigned)(lane % 7) * 8, epoch); *(volatile LAS unsigned*)(uintptr_t)L_GO = 3u;
            } else { while (*(const volatile LAS unsigned*)(uintptr_t)L_GO != 3u) __builtin_amdgcn_s_sleep(1); asm volatile("" ::: "memory"); }
            STAMP(6);
#pragma unroll
            for (int i = 0; i < 14; ++i) dma_sc1(rWs, lo, WS_H + ((tt0 + i / 7) * 28 + R4(4 * (i % 7) + w)) * 1024, L_A + ((i / 7) * 28 + 4 * (i % 7) + w) * KB);
            waitv<7>();
        } else {
            while (*(const volatile LAS unsigned*)(uintptr_t)L_GO != 3u) __builtin_amdgcn_s_sleep(1);
            asm volatile("" ::: "memory");
            STAMP(6);
#pragma unroll
            for (int i = 0; i < 7; ++i) stv[i] = __builtin_amdgcn_raw_buffer_load_b64(rWs, (unsigned)(hh * 256 + r * 8), WS_ST + ((tt0 + (w & 1)) * 28 + 4 * i + (w & 2)) * 256, 16);
        }
        BARX();
        STAMP(17);
        f32x16 a0 = {}, a1 = {};
        u32x4 bq[4];
        bq[0] = lds16(L_A + lo); bq[1] = lds16(L_A + KB + lo); bq[2] = lds16(L_A + 2 * KB + lo);
        SB();
        f32x2 sv[2][4];
        float nm[2], ce[2], rs1;
        u32x4 o[2];
        const unsigned hso = WS_H1 + (tt0 * 112 + cb * 16 + w * 2) * 1024;
#define W2LD(q) f2[q] = ld16(rWs, lo, w2b + (unsigned)(((q) & 1) * 112 + R5((q) >> 1)) * 1024)
        auto step = [&](auto sc) __attribute__((always_inline)) {
            constexpr int s = decltype(sc)::value;
            if (s == 16) {
                if (w >= 4) {
                    float s1 = 0.f, s2 = 0.f;
#pragma unroll
                    for (int i = 0; i < 7; ++i) { s1 = add1(s1, __uint_as_float(stv[i][0])); s2 = add1(s2, __uint_as_float(stv[i][1])); }
                    *(LAS f32x2*)(uintptr_t)(L_M + (w - 4) * 512 + lane * 8) = f32x2{s1, s2};
                } else waitv<4>();
                BAR();
            }
            if (s + 3 < 56) bq[(s + 3) & 3] = lds16(L_A + (s + 3) * KB + lo);
            if constexpr (s == 17 || s == 18) {
#pragma unroll
                for (int i = 0; i < 4; ++i) sv[s - 17][i] = *(const LAS f32x2*)(uintptr_t)(L_M + ((i >> 1) * 2 + (s - 17)) * 512 + ((i & 1) * 32 + r) * 8);
            }
            SB();
            if constexpr (s < 28) { a0 = MFMA(fr[s % 28], bq[s & 3], a0); PIN(a0); } else { a1 = MFMA(fr[s % 28], bq[s & 3], a1); PIN(a1); }
            SB();
            if constexpr (s < 28 && s % 4 == 3) W2LD(s / 4);
            if constexpr (s >= 28 && s % 3 == 0 && 7 + (s - 28) / 3 < NW2) W2LD(7 + (s - 28) / 3);
            if constexpr (s == 22 || s == 24) {
                const int m = (s - 22) / 2;
                const float s1 = add1(add1(sv[m][0][0], sv[m][1][0]), add1(sv[m][2][0], sv[m][3][0])), s2 = add1(add1(sv[m][0][1], sv[m][1][1]), add1(sv[m][2][1], sv[m][3][1]));
                const float mean = s1 * (1.f / DM), rs = __builtin_amdgcn_rsqf(fmaxf(s2 * (1.f / DM) - mean * mean, 0.f) + 1e-5f);
                nm[m] = -mean; ce[m] = rs * -1.4426950408889634f;
                if (m == 0) rs1 = rs; else rs1 = mt ? rs : rs1;
                PIN(nm[m]); PIN(ce[m]); PIN(rs1);
            }
            if constexpr (s == 32 || s == 37 || s == 42 || s == 47) {
                const int g = (s - 32) / 5;
                const u32x2 y = silu4(a0[4 * g], a0[4 * g + 1], a0[4 * g + 2], a0[4 * g + 3], cs[g], nm[0], ce[0]);
                o[g >> 1][(g & 1) * 2] = y[0]; o[g >> 1][(g & 1) * 2 + 1] = y[1];
                PIN(o[g >> 1]);
                if (g & 1) st16(rWs, o[g >> 1], lo + (g >> 1) * 1024, hso);
            }
            SB();
        };
        static_for<0, 56>(step);
        rstd2 = rs1;
        asm volatile("s_nop 15" : "+v"(a1));
        STAMP(18);
#pragma unroll
        for (int g = 0; g < 4; ++g) {
            const u32x2 y = silu4(a1[4 * g], a1[4 * g + 1], a1[4 * g + 2], a1[4 * g + 3], cs[g], nm[1], ce[1]);
            o[g >> 1][(g & 1) * 2] = y[0]; o[g >> 1][(g & 1) * 2 + 1] = y[1];
            if (g & 1) st16(rWs, o[g >> 1], lo + (g >> 1) * 1024, hso + 112 * 1024);
        }
        STAMP(19);
        waitv<0>();
    }
    STAMP(7);
    {
        BAR();
        if (tid == 0) set_flag(ws, WS_F4 + grp * 256 + cb * 8, epoch);
        SB();
#pragma unroll
        for (int q = NW2; q < 28; ++q) f2[q] = ld16(rWs, lo, w2b + (unsigned)((q & 1) * 112 + R5(q >> 1)) * 1024);
        if (w == 0) wait_flags(ws, WS_F4 + grp * 256 + (unsigned)(lane % 7) * 8, epoch);
        BARX();
        STAMP(8);
        u32x4 bf[28];
        const unsigned bs = WS_H1 + (tt0 * 112 + 14 * w) * 1024;
#define BLD(t) do { const unsigned o_ = bs + R5(t) * 1024; bf[2 * (t)] = ld16(rWs, lo, o_); bf[2 * (t) + 1] = ld16(rWs, lo, o_ + 112 * 1024); } while (0)
#pragma unroll
        for (int t = 0; t < NB5; ++t) BLD(t);
        SB();
        f32x16 ac[4] = {{}, {}, {}, {}};
#pragma unroll
        for (int t = 0; t < 14; ++t) {
            ac[0] = MFMA(f2[2 * t], bf[2 * t], ac[0]); ac[2] = MFMA(f2[2 * t + 1], bf[2 * t], ac[2]);
            ac[1] = MFMA(f2[2 * t], bf[2 * t + 1], ac[1]); ac[3] = MFMA(f2[2 * t + 1], bf[2 * t + 1], ac[3]);
            PIN(ac[0]); PIN(ac[1]); PIN(ac[2]); PIN(ac[3]);
            SB();
            if (t + NB5 < 14) BLD(t + NB5);
            SB();
            if (t == 0) STAMP(20); if (t == 13) STAMP(21);
        }
#pragma unroll
        for (int p = 0; p < 8; ++p) {
            const f32x16& o = ac[(p >> 2) * 2 + (p & 1)]; const int h8 = ((p >> 1) & 1) * 8;
            const unsigned dst = (unsigned)(p * 8 + w) * 2048 + lo;
            *(LAS f32x4*)(uintptr_t)dst = f32x4{o[h8], o[h8 + 1], o[h8 + 2], o[h8 + 3]};
            *(LAS f32x4*)(uintptr_t)(dst + 1024) = f32x4{o[h8 + 4], o[h8 + 5], o[h8 + 6], o[h8 + 7]};
        }
        BAR();
        f32x4 y0 = {0.f, 0.f, 0.f, 0.f}, y1 = y0;
#pragma unroll
        for (int i = 0; i < 8; ++i) { const unsigned src = (unsigned)(w * 8 + i) * 2048 + lo; y0 += lds16f(src); y1 += lds16f(src + 1024); }
        const u32x2 z0 = {pk(fmaf(y0[0], rstd2, bflo(hres[0])), fmaf(y0[1], rstd2, bfhi(hres[0]))), pk(fmaf(y0[2], rstd2, bflo(hres[1])), fmaf(y0[3], rstd2, bfhi(hres[1])))};
        const u32x2 z1 = {pk(fmaf(y1[0], rstd2, bflo(hres[2])), fmaf(y1[1], rstd2, bfhi(hres[2]))), pk(fmaf(y1[2], rstd2, bflo(hres[3])), fmaf(y1[3], rstd2, bfhi(hres[3])))};
        __builtin_amdgcn_raw_buffer_store_b64(z0, rOut, xo, 0, 0);
        __builtin_amdgcn_raw_buffer_store_b64(z1, rOut, xo + 16, 0, 0);
    }
    STAMP(9); STAMP(30);
}

extern "C" void kernel_launch(void* const* d_in, const int* in_sizes, int n_in, void* d_out, int out_size, void* d_ws, size_t ws_size,
                              hipStream_t stream) {
    (void)hipFuncSetAttribute((const void*)block_kernel, hipFuncAttributeMaxDynamicSharedMemorySize, LDS_BYTES);
    block_kernel<<<256, 512, LDS_BYTES, stream>>>(d_in[0], d_in[1], d_in[2], d_in[4], d_in[5], d_out, (char*)d_ws);
}
```

```cpp
#include <hip/hip_runtime.h>
#include <stdint.h>

typedef __attribute__((ext_vector_type(8))) __bf16 bf16x8;
typedef __attribute__((ext_vector_type(2))) __bf16 bf16x2;
typedef __attribute__((ext_vector_type(4))) short s16x4;
typedef __attribute__((ext_vector_type(16))) float f32x16;
typedef __attribute__((ext_vector_type(4))) float f32x4;
typedef __attribute__((ext_vector_type(2))) float f32x2;
typedef __attribute__((ext_vector_type(4))) unsigned u32x4;
typedef __attribute__((ext_vector_type(2))) unsigned u32x2;
typedef unsigned long long u64;
#define LAS __attribute__((address_space(3)))
#define GAS __attribute__((address_space(1)))
#define DI __device__ __forceinline__
typedef __amdgpu_buffer_rsrc_t rsrc_t;

constexpr int SEQ = 2048, DM = 448, NH = 16, DH = 28, FF = 1792;
constexpr unsigned WS_EPOCH = 0;
constexpr unsigned WS_F1 = 4096;
constexpr unsigned WS_FS = WS_F1 + 16 * 256;
constexpr unsigned WS_F2 = WS_FS + 1024;
constexpr unsigned WS_F3 = WS_F2 + 16 * 256;
constexpr unsigned WS_F4 = WS_F3 + 32 * 256;
constexpr unsigned WS_CS0 = 65536;
constexpr unsigned WS_CS1 = WS_CS0 + 21 * 256;
constexpr unsigned WS_STAMP = 32u << 20;
constexpr unsigned WS_Q = 131072;
constexpr unsigned WS_K = WS_Q + 64 * 28 * 1024;
constexpr unsigned WS_V = WS_K + 64 * 28 * 1024;
constexpr unsigned WS_O = WS_V + 64 * 28 * 1024;
constexpr unsigned WS_H = WS_O + 64 * 32 * 1024;
constexpr unsigned WS_ST = WS_H + 64 * 28 * 1024;
constexpr unsigned WS_H1 = WS_ST + 64 * 28 * 256;
constexpr unsigned WS_W1T = WS_H1 + 64 * 112 * 1024;
constexpr unsigned WS_W2T = WS_W1T + 56 * 28 * 1024;
constexpr unsigned WS_WOT = WS_W2T + 14 * 112 * 1024;
constexpr unsigned WS_END = WS_WOT + 14 * 28 * 1024;
constexpr unsigned KB = 1024;
constexpr unsigned L_XR = 0, L_WR = 64 * KB, L_WQ = 120 * KB, L_CS = 148 * KB, L_XS = L_CS + 1024;
constexpr unsigned L_A = 0, L_RING = 56 * KB, L_M = 152 * KB;
constexpr unsigned L_SCR = L_RING + 6 * 8 * KB;
constexpr unsigned L_CMB = 64 * KB;
constexpr unsigned L_RED = 64 * KB;
constexpr unsigned L_BAD = 159 * KB, L_CNT = L_BAD + 4, L_GO = L_BAD + 8;
constexpr unsigned LDS_BYTES = 160 * KB;
constexpr unsigned SPIN_CAP = 1u << 16;
constexpr unsigned CODE_PF = 32 * 1024;
constexpr unsigned OOB = 0x7f000000u;

#define WSZ(x) (x)
#define EWQ 1
#define EWM 1
#define NW1A 19
#define EWS 16
#define NW1B 23
#define NW2 16
#define LTS 1
#define NPK 12
#define SPS 64
#define NB5 5
#define MFMA(a, b, c) __builtin_amdgcn_mfma_f32_32x32x16_bf16(__builtin_bit_cast(bf16x8, a), __builtin_bit_cast(bf16x8, b), c, 0, 0, 0)
#define BAR() asm volatile("s_waitcnt lgkmcnt(0)\n\ts_barrier" ::: "memory")
#define BARX() asm volatile("s_barrier" ::: "memory")
#define LGK() asm volatile("s_waitcnt lgkmcnt(0)" ::: "memory")
#define SB() __builtin_amdgcn_sched_barrier(0)
#define PIN(x) asm volatile("" : "+v"(x))
#define EX2(x) __builtin_amdgcn_exp2f(x)
#define RLX_AGENT __ATOMIC_RELAXED, __HIP_MEMORY_SCOPE_AGENT

#define STAMP(i) do {} while (0)

template <int I> struct IC { static constexpr int value = I; };
template <int I, int E, class F> DI void static_for(F& f) { if constexpr (I < E) { f(IC<I>{}); static_for<I + 1, E>(f); } }
template <int N> DI void waitv() { asm volatile("s_waitcnt vmcnt(%0)" ::"n"(N) : "memory"); }
DI void dma(rsrc_t rs, unsigned voff, unsigned soff, unsigned lds) {
    asm volatile("s_mov_b32 m0, %0\n\ts_nop 0\n\tbuffer_load_dwordx4 %1, %2, %3 offen lds" ::"s"(lds), "v"(voff), "s"(rs), "s"(soff) : "memory");
}
DI void dma_sc1(rsrc_t rs, unsigned voff, unsigned soff, unsigned lds) {
    asm volatile("s_mov_b32 m0, %0\n\ts_nop 0\n\tbuffer_load_dwordx4 %1, %2, %3 offen sc1 lds" ::"s"(lds), "v"(voff), "s"(rs), "s"(soff) : "memory");
}
DI u32x4 ld16(rsrc_t rs, unsigned voff, unsigned soff) { return __builtin_amdgcn_raw_buffer_load_b128(rs, voff, soff, 16); }
DI void st16(rsrc_t rs, u32x4 v, unsigned voff, unsigned soff) { __builtin_amdgcn_raw_buffer_store_b128(v, rs, voff + soff, 0, 16); }
DI void st8(rsrc_t rs, u32x2 v, unsigned voff, unsigned soff) { __builtin_amdgcn_raw_buffer_store_b64(v, rs, voff, soff, 16); }
DI u32x4 lds16(unsigned a) { return *(const LAS u32x4*)(uintptr_t)a; }
DI f32x4 lds16f(unsigned a) { return *(const LAS f32x4*)(uintptr_t)a; }
DI u32x2 ldstr(unsigned a) { return __builtin_bit_cast(u32x2, __builtin_amdgcn_ds_read_tr16_b64_v4i16((LAS s16x4*)(uintptr_t)a)); }
template <int D2> DI u32x4 wfrag(unsigned a) { u32x2 lo = ldstr(a), hi = ldstr(a + D2); return u32x4{lo[0], lo[1], hi[0], hi[1]}; }
DI unsigned pk(float lo, float hi) { return __builtin_bit_cast(unsigned, __builtin_convertvector(f32x2{lo, hi}, bf16x2)); }
DI float bflo(unsigned u) { return __uint_as_float(u << 16); }
DI float bfhi(unsigned u) { return __uint_as_float(u & 0xffff0000u); }
DI float sum32(float v) {
    auto r = __builtin_amdgcn_permlane32_swap(__float_as_uint(v), __float_as_uint(v), false, false);
    return __uint_as_float(r[0]) + __uint_as_float(r[1]);
}
DI float max32(float v) {
    auto r = __builtin_amdgcn_permlane32_swap(__float_as_uint(v), __float_as_uint(v), false, false);
    return fmaxf(__uint_as_float(r[0]), __uint_as_float(r[1]));
}
DI int fresh_lane() { int l; asm volatile("v_mbcnt_lo_u32_b32 %0, -1, 0\n\tv_mbcnt_hi_u32_b32 %0, -1, %0" : "=v"(l)); return l; }
DI float add1(float a, float b) { float d; asm("v_add_f32 %0, %1, %2" : "=v"(d) : "v"(a), "v"(b)); return d; }
DI float mul1(float a, float b) { float d; asm("v_mul_f32 %0, %1, %2" : "=v"(d) : "v"(a), "v"(b)); return d; }
DI float fma1(float a, float b, float c) { float d; asm("v_fma_f32 %0, %1, %2, %3" : "=v"(d) : "v"(a), "v"(b), "v"(c)); return d; }
DI u32x2 silu4(float a0, float a1, float a2, float a3, f32x4 c, float nm, float ce) {
    unsigned o0, o1; float z0, z1, z2, z3, t0, t1, t2, t3;
    asm("v_fma_f32 %2, %14, %10, %16\n\tv_fma_f32 %3, %14, %11, %17\n\tv_fma_f32 %4, %14, %12, %18\n\tv_fma_f32 %5, %14, %13, %19\n\t"
        "v_mul_f32 %6, %15, %2\n\tv_mul_f32 %7, %15, %3\n\tv_mul_f32 %8, %15, %4\n\tv_mul_f32 %9, %15, %5\n\t"
        "v_exp_f32 %6, %6\n\tv_exp_f32 %7, %7\n\tv_exp_f32 %8, %8\n\tv_exp_f32 %9, %9\n\t"
        "v_add_f32 %6, 1.0, %6\n\tv_add_f32 %7, 1.0, %7\n\tv_add_f32 %8, 1.0, %8\n\tv_add_f32 %9, 1.0, %9\n\t"
        "v_rcp_f32 %6, %6\n\tv_rcp_f32 %7, %7\n\tv_rcp_f32 %8, %8\n\tv_rcp_f32 %9, %9\n\t"
        "v_mul_f32 %2, %2, %6\n\tv_mul_f32 %3, %3, %7\n\tv_mul_f32 %4, %4, %8\n\tv_mul_f32 %5, %5, %9\n\t"
        "v_cvt_pk_bf16_f32 %0, %2, %3\n\tv_cvt_pk_bf16_f32 %1, %4, %5"
        : "=v"(o0), "=v"(o1), "=&v"(z0), "=&v"(z1), "=&v"(z2), "=&v"(z3), "=&v"(t0), "=&v"(t1), "=&v"(t2), "=&v"(t3)
        : "v"(c[0]), "v"(c[1]), "v"(c[2]), "v"(c[3]), "v"(nm), "v"(ce), "v"(a0), "v"(a1), "v"(a2), "v"(a3));
    return u32x2{o0, o1};
}
DI float dot2(unsigned a, unsigned b, float c) { return __builtin_amdgcn_fdot2_f32_bf16(__builtin_bit_cast(bf16x2, a), __builtin_bit_cast(bf16x2, b), c, false); }

DI void wait_flags(char* ws, unsigned off, u64 epoch) {
    const GAS u64* p = (const GAS u64*)(ws + off);
    for (unsigned sp = 0; sp < SPIN_CAP; ++sp) {
        const u64 v = __hip_atomic_load(p, RLX_AGENT);
        if (__all(v == epoch)) break;
    }
    asm volatile("" ::: "memory");
}
DI void wait_flags2(char* ws, unsigned off0, unsigned off1, u64 epoch) {
    const GAS u64* p0 = (const GAS u64*)(ws + off0); const GAS u64* p1 = (const GAS u64*)(ws + off1);
    for (unsigned sp = 0; sp < SPIN_CAP; ++sp) {
        const u64 v0 = __hip_atomic_load(p0, RLX_AGENT), v1 = __hip_atomic_load(p1, RLX_AGENT);
        if (__all(v0 == epoch && v1 == epoch)) break;
    }
    asm volatile("" ::: "memory");
}
DI void set_flag(char* ws, unsigned off, u64 epoch) { __hip_atomic_store((GAS u64*)(ws + off), epoch, RLX_AGENT); }

__global__ void __launch_bounds__(512, 2)
block_kernel(const void* __restrict__ gx, const void* __restrict__ gwqkv, const void* __restrict__ gwo, const void* __restrict__ gw1,
             const void* __restrict__ gw2, void* __restrict__ gout, char* __restrict__ ws) {
    extern __shared__ __attribute__((aligned(16))) char smem[];
    const int tid = threadIdx.x;
    const int w = __builtin_amdgcn_readfirstlane(tid >> 6);
    const int b = blockIdx.x, xcd = b & 7, bi = b >> 3;
    const rsrc_t rX = __builtin_amdgcn_make_buffer_rsrc((void*)gx, 0, SEQ * DM * 2, 0x00020000);
    const rsrc_t rWqkv = __builtin_amdgcn_make_buffer_rsrc((void*)gwqkv, 0, DM * 3 * DM * 2, 0x00020000);
    const rsrc_t rW1 = __builtin_amdgcn_make_buffer_rsrc((void*)gw1, 0, WSZ(DM * FF * 2), 0x00020000);
    const rsrc_t rW2 = __builtin_amdgcn_make_buffer_rsrc((void*)gw2, 0, WSZ(FF * DM * 2), 0x00020000);
    const rsrc_t rOut = __builtin_amdgcn_make_buffer_rsrc(gout, 0, SEQ * DM * 2, 0x00020000);
    const rsrc_t rWs = __builtin_amdgcn_make_buffer_rsrc(ws, 0, WS_END, 0x00020000);
    const int tb1 = 4 * (xcd >> 1) + bi / 7, c1 = 7 * (xcd & 1) + bi % 7;
    const int h2 = 2 * xcd + (bi & 1), qb = bi >> 1;
    const int tb3 = 2 * xcd + (bi >> 4), j3 = bi & 15, mh = j3 & 1, cb = j3 >> 1;
    const bool act3 = j3 < 14;
    const int g4 = (bi >> 4) * 2 + mh, r4 = 7 * g4;
#define R4(s) ((s) + r4 >= 28 ? (s) + r4 - 28 : (s) + r4)
    u32x4 fa[16];
    u32x4 fe[NPK];
    const unsigned w1b = act3 ? WS_W1T + (unsigned)(cb * 8 + w) * 28 * 1024 : OOB;
    STAMP(31); STAMP(0);
    const u64 epoch = *(const __attribute__((address_space(4))) u64*)(ws + WS_EPOCH) + 1;
    {
    const int lane = tid & 63, r = lane & 31, hh = lane >> 5;
    const unsigned trn = (unsigned)((8 * hh + ((lane & 15) >> 2)) * 64 + ((lane >> 4) & 1) * 32 + (lane & 3) * 8);
    const unsigned trp = (unsigned)((4 * hh + ((lane & 15) >> 2)) * 64 + ((lane >> 4) & 1) * 32 + (lane & 3) * 8);
    const unsigned v1 = (lane >> 2) * 3584 + (lane & 3) * 16;
    const unsigned v2 = (lane >> 2) * 896 + (lane & 3) * 16;
    const unsigned lo = lane * 16;
    if (tid == 0) *(LAS u32x4*)(uintptr_t)L_BAD = u32x4{0u, 0u, 0u, 0u};

    if (w == 0) dma(rX, lane < 56 ? lane * 128 : OOB, (unsigned)b * 7168, L_M + 4096);
    if (w == 1) dma(rWqkv, lane < 37 ? lane * 128 : OOB, (unsigned)b * 4704, L_M + 4096);
#define LATE_TOUCH(j) do { \
        if ((j) == 0) dma_sc1(rWs, lane < 16 ? lane * 2097152 : lane == 16 ? WS_F1 + c1 * 256 : lane == 17 ? WS_F2 + qb * 256 : lane == 18 ? WS_F3 + (tb3 * 2 + mh) * 256 : lane == 19 ? WS_F4 + (tb3 * 2 + mh) * 256 : lane == 20 ? WS_F1 + c1 * 256 + 128 : OOB, 0, L_M + 4096); \
        else if ((j) == 1) dma(rOut, lane < 7 ? lane * 262144 : OOB, 0, L_M + 4096); \
        else dma(rOut, OOB, 0, L_M + 4096); } while (0)
    if (w == 2) {
        u64 pc; asm volatile("s_getpc_b64 %0" : "=s"(pc));
        dma(__builtin_amdgcn_make_buffer_rsrc((void*)(pc & ~127ull), 0, CODE_PF, 0x00020000), lane < 10 ? lane * 128 : OOB, (unsigned)bi * 1280, L_M + 4096);
    }
    if (bi >= 28) {
        const int id = (bi - 28) * 8 + xcd;
        const unsigned so = id < 28 ? (unsigned)(w * 16 * 3584 + id * 128) : OOB, ld = w * 8 * KB;
#pragma unroll
        for (int i = 0; i < 4; ++i) { dma(rW1, v1, so + i * 128 * 3584, ld + 2 * i * KB); dma(rW1, v1, so + i * 128 * 3584 + 64, ld + (2 * i + 1) * KB); }
        const unsigned dst = id < 28 ? WS_W1T + (unsigned)(id * 56 + w) * 1024 : OOB;
        STAMP(10);
        waitv<0>();
        STAMP(11);
        float c[2] = {0.f, 0.f};
#pragma unroll
        for (int i = 0; i < 8; ++i) {
            const u32x4 f = wfrag<512>(ld + trp + i * KB);
            st16(rWs, f, lo, (i >= 6 && w >= 4) ? OOB : dst + ((i & 1) * 28 + (i >> 1) * 8) * 1024);
#pragma unroll
            for (int e = 0; e < 4; ++e) c[i & 1] += bflo(f[e]) + bfhi(f[e]);
        }
        STAMP(12);
        c[0] = sum32(c[0]); c[1] = sum32(c[1]);
        *(LAS float*)(uintptr_t)(L_M + w * 256 + lane * 4) = hh ? c[1] : c[0];
        BAR();
        if (w == 0) {
            float t = 0.f;
#pragma unroll
            for (int i = 0; i < 8; ++i) t += *(const LAS float*)(uintptr_t)(L_M + i * 256 + lane * 4);
            __builtin_amdgcn_raw_buffer_store_b32(__float_as_uint(t), rWs, lane * 4, id < 28 ? WS_CS1 + (unsigned)id * 256 : OOB, 16);
        }
        waitv<0>();
        STAMP(13);
        BAR();
        if (tid == 0) set_flag(ws, WS_FS + id * 8, epoch);
        dma(__builtin_amdgcn_make_buffer_rsrc((void*)gwo, 0, DM * DM * 2, 0x00020000), lane < 13 ? lane * 128 : OOB, (unsigned)(id * 8 + w) * 1568, L_M + 4096);
        dma(rW2, lane < 49 ? lane * 128 : OOB, (unsigned)(id * 8 + w) * 6272, L_M + 4096);
        if (w >= 4) LATE_TOUCH(w - 4);
    } else {
      const int mt1 = w & 3;
      const unsigned xl = L_XR + mt1 * 16 * KB, kt = (unsigned)(tb1 * 4 + mt1);
      const unsigned xb = xl + (r >> 3) * KB + (r & 7) * 128 + ((hh ^ ((r >> 1) & 7)) * 16);
      if (w < 4) {
        const int rin = lane >> 3, sl = lane & 7;
        const unsigned ve = rin * 896 + ((sl ^ (rin >> 1)) * 16), vo = rin * 896 + ((sl ^ (4 + (rin >> 1))) * 16);
        const unsigned xs = (unsigned)(tb1 * 128 + w * 32) * 896;
#define XDMA(c) do { _Pragma("unroll") for (int g = 0; g < 4; ++g) dma(rX, (g & 1) ? vo : ve, xs + (c) * 128 + g * 8 * 896, xl + ((c) & 3) * 4 * KB + g * KB); } while (0)
        XDMA(0); XDMA(1);
        u32x4 xf[8];
#define XRD(c) do { _Pragma("unroll") for (int s = 0; s < 4; ++s) xf[4 * ((c) & 1) + s] = lds16((xb ^ (s * 32)) + ((c) & 3) * 4 * KB); } while (0)
        f32x16 ac[2] = {{}, {}};
        unsigned wbA = L_WR + trn;
        PIN(wbA);
        u32x4 fw[3][2];
#define WRD(ks) do { fw[(ks) % 3][0] = wfrag<256>(wbA + (ks) * 2 * KB); fw[(ks) % 3][1] = wfrag<256>(wbA + (ks) * 2 * KB + KB); } while (0)
        float s1 = 0.f, s2 = 0.f;
#define XST(c) do { _Pragma("unroll") for (int q = 0; q < 4; ++q) _Pragma("unroll") for (int e = 0; e < 4; ++e) { s1 = dot2(xf[4 * ((c) & 1) + q][e], 0x3F803F80u, s1); s2 = dot2(xf[4 * ((c) & 1) + q][e], xf[4 * ((c) & 1) + q][e], s2); } PIN(s1); PIN(s2); SB(); } while (0)
        waitv<4>(); XRD(0); XDMA(2); XST(0);
        BARX(); WRD(0); WRD(1); SB();
#pragma unroll
        for (int ks = 0; ks < 28; ++ks) {
            ac[0] = MFMA(fw[ks % 3][0], xf[ks & 7], ac[0]); PIN(ac[0]);
            SB();
            if (ks + 2 < 28) {
                if ((ks + 2) % 4 == 0) {
                    const int c = (ks + 2) / 4;
                    if (c < 6) waitv<4>(); else waitv<0>();
                    XRD(c);
                    if (c + 2 < 7) XDMA(c + 2);
                    BARX();
                }
                WRD(ks + 2);
                if ((ks + 2) % 4 == 1 && ks > 2) XST((ks + 2) / 4);
            }
            SB();
            ac[1] = MFMA(fw[ks % 3][1], xf[ks & 7], ac[1]); PIN(ac[1]);
            SB();
        }
        STAMP(12);
        s1 = sum32(s1); s2 = sum32(s2);
        const float mean = s1 * (1.f / DM), rstd = __builtin_amdgcn_rsqf(fmaxf(s2 * (1.f / DM) - mean * mean, 0.f) + 1e-5f), ms = -mean * rstd;
        if (hh == 0) *(LAS f32x2*)(uintptr_t)(L_XS + (w * 32 + r) * 8) = f32x2{mean, rstd};
        BAR();
        STAMP(13);
        f32x4 csr[2][4];
#pragma unroll
        for (int t = 0; t < 2; ++t)
#pragma unroll
            for (int g = 0; g < 4; ++g) csr[t][g] = lds16f(L_CS + t * 128 + (8 * g + 4 * hh) * 4);
        u32x2 pkd[2][4];
#pragma unroll
        for (int t = 0; t < 2; ++t)
#pragma unroll
            for (int g = 0; g < 4; ++g) {
                float y[4];
#pragma unroll
                for (int e = 0; e < 4; ++e) y[e] = fma1(ac[t][4 * g + e], rstd, mul1(ms, csr[t][g][e]));
                pkd[t][g] = u32x2{pk(y[0], y[1]), pk(y[2], y[3])};
            }
        if (c1 < 7) {
            const unsigned base = WS_K + kt * 28672 + (unsigned)c1 * 4096;
#pragma unroll
            for (int q = 0; q < 4; ++q) {
                const int t = q >> 1, g = (q & 1) * 2;
                const auto x0 = __builtin_amdgcn_permlane32_swap(pkd[t][g][0], pkd[t][g + 1][0], false, false), x1 = __builtin_amdgcn_permlane32_swap(pkd[t][g][1], pkd[t][g + 1][1], false, false);
                st16(rWs, u32x4{x0[0], x1[0], x0[1], x1[1]}, lo + q * 1024, base);
            }
        } else {
            const unsigned vt = L_WR + w * 4 * KB;
#pragma unroll
            for (int t = 0; t < 2; ++t)
#pragma unroll
                for (int g = 0; g < 4; ++g) *(LAS u32x2*)(uintptr_t)(vt + t * 2 * KB + r * 64 + (2 * g + hh) * 8) = pkd[t][g];
            asm volatile("" ::: "memory");
            const unsigned off = (unsigned)(hh * 7168 + ((c1 - 7) * 64 + r) * 16);
#pragma unroll
            for (int t = 0; t < 2; ++t) {
                const u32x4 f0 = wfrag<512>(vt + t * 2 * KB + trp), f1 = wfrag<512>(vt + t * 2 * KB + trp + 1024);
                st16(rWs, f0, off + t * 512, WS_V + kt * 28672); st16(rWs, f1, off + t * 512 + 14336, WS_V + kt * 28672);
            }
        }
        STAMP(14);
        BARX();
        waitv<0>();
        if (lane == 0 && __hip_atomic_fetch_add((LAS unsigned*)(uintptr_t)L_CNT, 1u, __ATOMIC_RELAXED, __HIP_MEMORY_SCOPE_WORKGROUP) == 3u) set_flag(ws, WS_F1 + c1 * 256 + tb1 * 8, epoch);
      } else {
        const int j = w - 4;
        const unsigned vw = (lane >> 2) * 2688 + (lane & 3) * 16;
        const unsigned so = (unsigned)(j * 16 * 2688 + 896 + c1 * 128), ld = L_WR + j * 2 * KB;
#define WDMA(st) do { dma(rWqkv, vw, so + (st) * 64 * 2688, ld + (st) * 8 * KB); dma(rWqkv, vw, so + (st) * 64 * 2688 + 64, ld + (st) * 8 * KB + KB); } while (0)
#define QDMA(st) dma(rWqkv, vw, (unsigned)((4 * (st) + j) * 16 * 2688 + c1 * 64), L_WQ + (4 * (st) + j) * KB)
        WDMA(0); WDMA(1); WDMA(2);
        const u32x4 ones = {0x3F803F80u, 0x3F803F80u, 0x3F803F80u, 0x3F803F80u};
        f32x16 cacc = {}, aq = {};
        u32x4 xq[28];
        unsigned wb = L_WR + trn + (j & 1) * KB;
        PIN(wb);
#pragma unroll
        for (int st = 0; st < 7; ++st) {
            if (st < 5) { if (st > LTS && st <= LTS + 3) waitv<5>(); else waitv<4>(); } else if (st == 5) waitv<5>(); else waitv<7>();
            if (st < 4) WDMA(st + 3);
            if (st == 4) { QDMA(0); QDMA(1); QDMA(2); }
            if (st == 5) { QDMA(3); QDMA(4); QDMA(5); QDMA(6); }
            if (st == LTS) LATE_TOUCH(j);
            BAR();
#pragma unroll
            for (int q = 0; q < 4; ++q) xq[4 * st + q] = lds16((xb ^ (q * 32)) + (st & 3) * 4 * KB);
            if (j < 2) {
                u32x4 f[4];
#pragma unroll
                for (int q = 0; q < 4; ++q) f[q] = wfrag<256>(wb + (4 * st + q) * 2 * KB);
#pragma unroll
                for (int q = 0; q < 4; ++q) cacc = MFMA(ones, f[q], cacc);
                PIN(cacc);
            }
            SB();
            if (st == 0) STAMP(10); if (st == 3) STAMP(11);
        }
        if (j < 2 && hh == 0) *(LAS float*)(uintptr_t)(L_CS + j * 128 + r * 4) = cacc[0];
        waitv<0>();
        BAR();
        unsigned qbs = L_WQ + trn;
        PIN(qbs);
        u32x4 fq[4];
        fq[0] = wfrag<256>(qbs); fq[1] = wfrag<256>(qbs + KB); fq[2] = wfrag<256>(qbs + 2 * KB);
        const f32x2 xs2 = *(const LAS f32x2*)(uintptr_t)(L_XS + (j * 32 + r) * 8);
        {
            cacc = f32x16{};
            u32x4 f[7];
#pragma unroll
            for (int i = 0; i < 7; ++i) f[i] = wfrag<256>(qbs + j * 7 * KB + i * KB);
#pragma unroll
            for (int i = 0; i < 7; ++i) cacc = MFMA(ones, f[i], cacc);
            PIN(cacc);
        }
        SB();
#pragma unroll
        for (int ks = 0; ks < 28; ++ks) {
            if (ks + 3 < 28) fq[(ks + 3) & 3] = wfrag<256>(qbs + (ks + 3) * KB);
            SB();
            aq = MFMA(fq[ks & 3], xq[ks], aq); PIN(aq);
            if (ks == 4 && hh == 0) *(LAS float*)(uintptr_t)(L_CS + 256 + j * 128 + r * 4) = cacc[0];
            SB();
        }
        BAR();
        const float sc = xs2[1] * (0.18898223650461362f * 1.4426950408889634f), ms = -xs2[0] * sc;
        u32x2 pkd[4];
#pragma unroll
        for (int g = 0; g < 4; ++g) {
            const unsigned ca_ = L_CS + 256 + (8 * g + 4 * hh) * 4;
            const f32x4 c = (lds16f(ca_) + lds16f(ca_ + 128)) + (lds16f(ca_ + 256) + lds16f(ca_ + 384));
            pkd[g] = u32x2{pk(fmaf(aq[4 * g], sc, ms * c[0]), fmaf(aq[4 * g + 1], sc, ms * c[1])), pk(fmaf(aq[4 * g + 2], sc, ms * c[2]), fmaf(aq[4 * g + 3], sc, ms * c[3]))};
        }
#pragma unroll
        for (int g = 0; g < 4; g += 2) {
            const auto x0 = __builtin_amdgcn_permlane32_swap(pkd[g][0], pkd[g + 1][0], false, false), x1 = __builtin_amdgcn_permlane32_swap(pkd[g][1], pkd[g + 1][1], false, false);
            st16(rWs, u32x4{x0[0], x1[0], x0[1], x1[1]}, lo + g * 512, WS_Q + kt * 28672 + (unsigned)c1 * 2048);
        }
        waitv<0>();
        if (lane == 0 && __hip_atomic_fetch_add((LAS unsigned*)(uintptr_t)(L_CNT + 8), 1u, __ATOMIC_RELAXED, __HIP_MEMORY_SCOPE_WORKGROUP) == 3u) set_flag(ws, WS_F1 + c1 * 256 + 128 + tb1 * 8, epoch);
      }
    }
    STAMP(1); STAMP(25);

    {
        const int qh = w & 1, kq = w >> 1;
        __builtin_amdgcn_s_setprio(3);
        const unsigned ch = (unsigned)((28 * h2) >> 3) * 512;
        const unsigned qso = WS_Q + (unsigned)(qb * 4 + qh * 2) * 28672 + ch;
        const rsrc_t rK = __builtin_amdgcn_make_buffer_rsrc(ws + WS_K + (unsigned)kq * 16 * 28672 + ch, 0, 15 * 28672 + 2048, 0x00020000);
        const rsrc_t rV = __builtin_amdgcn_make_buffer_rsrc(ws + WS_V + (unsigned)kq * 16 * 28672 + (unsigned)h2 * 448, 0, 16 * 28672 - h2 * 448, 0x00020000);
        unsigned vo0 = (unsigned)(hh * 7168 + r * 16), vo1 = vo0 + 14336;
        const unsigned vpad = r == DH ? 0x3F803F80u : 0u;
        unsigned qm0 = ((h2 & 1) && !hh) ? 0u : ~0u, qm1 = (!(h2 & 1) && hh) ? 0u : ~0u;
        u32x4 kf[4][2], vf[4][2], pa[2], pb[2], qa0, qa1, qb0, qb1;
        f32x16 oa = {}, ob = {}, sa, sb = {}, ca, cbv;
#pragma unroll
        for (int i = 0; i < 4; ++i) { vf[i][0] = vf[i][1] = u32x4{vpad, vpad, vpad, vpad}; PIN(vf[i][0]); PIN(vf[i][1]); }
        pb[0] = pb[1] = u32x4{0u, 0u, 0u, 0u};
#pragma unroll
        for (int i = 0; i < NPK; ++i) fe[i] = pb[0];
        PIN(oa); PIN(ob); PIN(sb); PIN(pb[0]); PIN(pb[1]); PIN(vo0); PIN(vo1); PIN(qm0); PIN(qm1);
        SB();
        if (w == 0) {
            const int c0 = (28 * h2) & ~7;
            const int col = lane < 32 ? c0 + 31 * ((lane >> 4) & 1) : 448 + 28 * h2 + 27 * ((lane >> 4) & 1);
            wait_flags2(ws, WS_F1 + (unsigned)(col >> 6) * 256 + (lane & 15) * 8, lane < 32 ? WS_F1 + (unsigned)((c0 + 31 * (lane & 1)) >> 5) * 256 + 128 + qb * 8 : WS_FS + (unsigned)r * 8, epoch);
            *(volatile LAS unsigned*)(uintptr_t)L_GO = 1u;
        } else {
            if (w >= 4 && act3) {
                const rsrc_t rWo = __builtin_amdgcn_make_buffer_rsrc((void*)gwo, 0, DM * DM * 2, 0x00020000);
                __builtin_amdgcn_s_sleep(EWS);
#pragma unroll
                for (int i = 0; i < 8; ++i) { const int id = 4 * i + w - 4; const unsigned so = (unsigned)(((id >> 1) * 28 + (id & 1) * 16) * 896 + cb * 128); dma(rWo, v2, so, L_A + id * 2 * KB); dma(rWo, v2, so + 64, L_A + id * 2 * KB + KB); }
            }
            while (*(const volatile LAS unsigned*)(uintptr_t)L_GO == 0u) __builtin_amdgcn_s_sleep(1);
            asm volatile("" ::: "memory");
        }
        STAMP(2); STAMP(26);
        asm volatile("buffer_load_dwordx4 %0, %6, %7, %8 offen sc1\n\tbuffer_load_dwordx4 %1, %6, %7, %8 offen offset:1024 sc1\n\t"
                     "buffer_load_dwordx4 %4, %6, %7, %10 offen sc1\n\tbuffer_load_dwordx4 %5, %6, %7, %10 offen offset:1024 sc1\n\t"
                     "buffer_load_dwordx4 %2, %6, %7, %9 offen sc1\n\tbuffer_load_dwordx4 %3, %6, %7, %9 offen offset:1024 sc1"
                     : "=&v"(qa0), "=&v"(qa1), "=&v"(qb0), "=&v"(qb1), "=&v"(pa[0]), "=&v"(pa[1]) : "v"(lo), "s"(rWs), "s"(qso), "s"(qso + 28672), "s"(WS_K + ch) : "memory");
#define KVLD(KC, K0, K1, V0, V1, SK, SV) asm volatile("buffer_load_dwordx4 %0, %4, %7, %9 offen sc1\n\tbuffer_load_dwordx4 %1, %4, %7, %9 offen offset:1024 sc1\n\t" \
                "s_mov_b64 exec, %11\n\tbuffer_load_dwordx4 %2, %5, %8, %10 offen sc1\n\tbuffer_load_dwordx4 %3, %6, %8, %10 offen sc1\n\ts_mov_b64 exec, -1" \
                : KC(K0), KC(K1), "+v"(V0), "+v"(V1) : "v"(lo), "v"(vo0), "v"(vo1), "s"(rK), "s"(rV), "s"(SK), "s"(SV), "s"(0x0fffffff0fffffffull) : "memory")
#pragma unroll
        for (int i = 0; i < 4; ++i) KVLD("=&v", kf[i][0], kf[i][1], vf[(i + 3) & 3][0], vf[(i + 3) & 3][1], (unsigned)(i * 28672), i ? (unsigned)((i - 1) * 28672) : OOB);
        asm volatile("s_waitcnt vmcnt(16)" : "+v"(qa0), "+v"(qa1), "+v"(qb0), "+v"(qb1), "+v"(pa[0]), "+v"(pa[1]) :: "memory");
        qa0[0] &= qm0; qa0[1] &= qm0; qb0[0] &= qm0; qb0[1] &= qm0; qa1[2] &= qm1; qa1[3] &= qm1; qb1[2] &= qm1; qb1[3] &= qm1;
        {
            const f32x16 z = {};
            ca = MFMA(pa[0], qa0, z); cbv = MFMA(pa[0], qb0, z); ca = MFMA(pa[1], qa1, ca); cbv = MFMA(pa[1], qb1, cbv);
            float ma = ca[0], mb = cbv[0];
#pragma unroll
            for (int i = 1; i < 16; ++i) { ma = fmaxf(ma, ca[i]); mb = fmaxf(mb, cbv[i]); }
            ma = -max32(ma); mb = -max32(mb);
#pragma unroll
            for (int i = 0; i < 16; ++i) { ca[i] = ma; cbv[i] = mb; }
        }
        PIN(ca); PIN(cbv);
        asm volatile("s_waitcnt vmcnt(12)" : "+v"(kf[0][0]), "+v"(kf[0][1]), "+v"(vf[3][0]), "+v"(vf[3][1]) :: "memory");
        sa = MFMA(kf[0][0], qa0, ca); sa = MFMA(kf[0][1], qa1, sa);
        PIN(sa); STAMP(15);
        SB();
#define GRP(MF, S, i, T, j, P) do { MF; SB(); \
        S[4 * (i)] = EX2(S[4 * (i)]); S[4 * (i) + 1] = EX2(S[4 * (i) + 1]); S[4 * (i) + 2] = EX2(S[4 * (i) + 2]); S[4 * (i) + 3] = EX2(S[4 * (i) + 3]); SB(); \
        P[(j) >> 1][((j) & 1) * 2] = pk(T[4 * (j)], T[4 * (j) + 1]); P[(j) >> 1][((j) & 1) * 2 + 1] = pk(T[4 * (j) + 2], T[4 * (j) + 3]); PIN(P[(j) >> 1]); PIN(S); SB(); } while (0)
#pragma unroll 1
        for (int tq = 0; tq < 4; ++tq) {
            const unsigned sk = (unsigned)tq * 4 * 28672;
#define FELD(i) asm volatile("buffer_load_dwordx4 %0, %1, %2, %3 offen sc1" : "+v"(fe[i]) : "v"(lo), "s"(rWs), "s"(w1b + R4(i) * 1024) : "memory")
            if (tq == 1) { FELD(0); FELD(1); FELD(2); FELD(3); }
            if (tq == 2) { FELD(4); FELD(5); FELD(6); FELD(7); }
            if (tq == 3) { FELD(8); FELD(9); FELD(10); FELD(11); }
            if (tq == 1) __builtin_amdgcn_s_setprio(2); if (tq == 2) __builtin_amdgcn_s_setprio(1); if (tq == 3) __builtin_amdgcn_s_setprio(0);
#pragma unroll
            for (int u = 0; u < 4; ++u) {
                GRP(ob = MFMA(vf[(u + 3) & 3][0], pb[0], ob), sa, 0, sb, 3, pb);
                GRP(sb = MFMA(kf[u][0], qb0, cbv), sa, 1, sa, 0, pa);
                GRP(sb = MFMA(kf[u][1], qb1, sb), sa, 2, sa, 1, pa);
                GRP(ob = MFMA(vf[(u + 3) & 3][1], pb[1], ob), sa, 3, sa, 2, pa);
                KVLD("+v", kf[u][0], kf[u][1], vf[(u + 3) & 3][0], vf[(u + 3) & 3][1], sk + (u + 4) * 28672, sk + (u + 3) * 28672);
                asm volatile("s_waitcnt vmcnt(12)" : "+v"(kf[(u + 1) & 3][0]), "+v"(kf[(u + 1) & 3][1]), "+v"(vf[u][0]), "+v"(vf[u][1]) :: "memory");
                SB();
                GRP(oa = MFMA(vf[u][0], pa[0], oa), sb, 0, sa, 3, pa);
                GRP(sa = MFMA(kf[(u + 1) & 3][0], qa0, ca), sb, 1, sb, 0, pb);
                GRP(sa = MFMA(kf[(u + 1) & 3][1], qa1, sa), sb, 2, sb, 1, pb);
                GRP(oa = MFMA(vf[u][1], pa[1], oa), sb, 3, sb, 2, pb);
            }
        }
        asm volatile("s_waitcnt vmcnt(0)" : "+v"(kf[0][0]), "+v"(kf[0][1]), "+v"(kf[1][0]), "+v"(kf[1][1]), "+v"(kf[2][0]), "+v"(kf[2][1]), "+v"(kf[3][0]), "+v"(kf[3][1]),
                     "+v"(vf[0][0]), "+v"(vf[0][1]), "+v"(vf[1][0]), "+v"(vf[1][1]), "+v"(vf[2][0]), "+v"(vf[2][1]) :: "memory");
#pragma unroll
        for (int i = 0; i < NPK; ++i) PIN(fe[i]);
        pb[1][2] = pk(sb[12], sb[13]); pb[1][3] = pk(sb[14], sb[15]);
        ob = MFMA(vf[3][0], pb[0], ob); ob = MFMA(vf[3][1], pb[1], ob);
        PIN(ob); STAMP(16);
        const int lane = fresh_lane(), r = lane & 31, hh = lane >> 5;
        const unsigned lo = lane * 16;
        unsigned cw = L_CMB + (unsigned)(qh * 16 + kq) * 2048 + lo;
        PIN(cw);
#pragma unroll 1
        for (int pass = 0; pass < 2; ++pass) {
#pragma unroll
            for (int p = 0; p < 4; ++p) {
                const f32x16& o = (p >> 1) ? ob : oa;
                *(LAS f32x4*)(uintptr_t)(cw + p * 8192) = f32x4{o[8 * (p & 1)], o[8 * (p & 1) + 1], o[8 * (p & 1) + 2], o[8 * (p & 1) + 3]};
                *(LAS f32x4*)(uintptr_t)(cw + p * 8192 + 1024) = f32x4{o[8 * (p & 1) + 4], o[8 * (p & 1) + 5], o[8 * (p & 1) + 6], o[8 * (p & 1) + 7]};
            }
            unsigned so0 = WS_O + (unsigned)(((qb * 4 + qh * 2 + (kq >> 1)) * 16 + h2) * 2 + (kq & 1)) * 1024 + lo;
            unsigned cr = L_CMB + (unsigned)(qh * 4 + kq) * 8192 + lo, cl = L_CMB + (unsigned)(qh * 4 + (kq | 1)) * 8192 + 1024 + (32 + r) * 16;
            PIN(so0); PIN(cr); PIN(cl);
            BAR();
            STAMP(22);
            f32x4 y0 = lds16f(cr), y1 = lds16f(cr + 1024);
            float l = *(const LAS float*)(uintptr_t)cl;
#pragma unroll
            for (int i = 1; i < 4; ++i) { y0 += lds16f(cr + i * 2048); y1 += lds16f(cr + i * 2048 + 1024); l += *(const LAS float*)(uintptr_t)(cl + i * 2048); }
            const float il = __builtin_amdgcn_rcpf(l);
            const float i1 = ((kq & 1) && hh) ? 0.f : il;
            const auto x0 = __builtin_amdgcn_permlane32_swap(pk(y0[0] * il, y0[1] * il), pk(y1[0] * i1, y1[1] * i1), false, false);
            const auto x1 = __builtin_amdgcn_permlane32_swap(pk(y0[2] * il, y0[3] * il), pk(y1[2] * i1, y1[3] * i1), false, false);
            st16(rWs, u32x4{x0[0], x1[0], x0[1], x1[1]}, so0, 0);
            const float t = ((fabsf(y0[0]) + fabsf(y0[1])) + (fabsf(y0[2]) + fabsf(y0[3]))) + ((fabsf(y1[0]) + fabsf(y1[1])) + (fabsf(y1[2]) + fabsf(y1[3]))) + l;
            if (__any(!(t < 1.0e36f))) *(LAS unsigned*)(uintptr_t)L_BAD = 1;
            if (act3) {
                const unsigned fb = L_A + (unsigned)((8 * hh + ((lane & 15) >> 2)) * 64 + ((lane >> 4) & 1) * 32 + (lane & 3) * 8) + kq * 16 * KB;
#pragma unroll
                for (int i = 0; i < 16; ++i) fa[i] = wfrag<256>(fb + i * KB);
            }
            waitv<0>();
            BAR();
            if (pass == 1 || !__builtin_amdgcn_readfirstlane(*(const volatile LAS unsigned*)(uintptr_t)L_BAD)) break;
            const f32x16 z = {};
            float ma = -INFINITY, mb = -INFINITY;
#pragma unroll 1
            for (int t = 0; t < 16; ++t) {
                const u32x4 k0 = ld16(rK, lo, t * 28672), k1 = ld16(rK, lo + 1024, t * 28672);
                f32x16 ta = MFMA(k0, qa0, z), tb = MFMA(k0, qb0, z); ta = MFMA(k1, qa1, ta); tb = MFMA(k1, qb1, tb);
#pragma unroll
                for (int i = 0; i < 16; ++i) { ma = fmaxf(ma, ta[i]); mb = fmaxf(mb, tb[i]); }
            }
            ma = max32(ma); mb = max32(mb);
            *(LAS float*)(uintptr_t)(L_M + 2048 + ((kq * 2 + qh) * 2 + hh) * 128 + r * 4) = hh ? mb : ma;
            BAR();
#pragma unroll
            for (int i = 0; i < 4; ++i) { ma = fmaxf(ma, *(const LAS float*)(uintptr_t)(L_M + 2048 + ((i * 2 + qh) * 2) * 128 + r * 4)); mb = fmaxf(mb, *(const LAS float*)(uintptr_t)(L_M + 2048 + ((i * 2 + qh) * 2 + 1) * 128 + r * 4)); }
            oa = z; ob = z;
            const unsigned vp = r == DH ? 0x3F803F80u : 0u, vm = r < DH ? ~0u : 0u, vo = (unsigned)(hh * 7168 + (r < DH ? r : 0) * 16);
#pragma unroll 1
            for (int t = 0; t < 16; ++t) {
                const u32x4 k0 = ld16(rK, lo, t * 28672), k1 = ld16(rK, lo + 1024, t * 28672);
                const u32x4 v0 = (ld16(rV, vo, t * 28672) & vm) | vp, v1 = (ld16(rV, vo + 14336, t * 28672) & vm) | vp;
                f32x16 ta = MFMA(k0, qa0, z), tb = MFMA(k0, qb0, z); ta = MFMA(k1, qa1, ta); tb = MFMA(k1, qb1, tb);
                u32x4 p0, p1, p2, p3;
#pragma unroll
                for (int i = 0; i < 4; ++i) {
                    p0[i] = pk(EX2(ta[2 * i] - ma), EX2(ta[2 * i + 1] - ma)); p1[i] = pk(EX2(ta[8 + 2 * i] - ma), EX2(ta[9 + 2 * i] - ma));
                    p2[i] = pk(EX2(tb[2 * i] - mb), EX2(tb[2 * i + 1] - mb)); p3[i] = pk(EX2(tb[8 + 2 * i] - mb), EX2(tb[9 + 2 * i] - mb));
                }
                oa = MFMA(v0, p0, oa); oa = MFMA(v1, p1, oa); ob = MFMA(v0, p2, ob); ob = MFMA(v1, p3, ob);
            }
        }
    }
    }
    const int lane = fresh_lane(), r = lane & 31, hh = lane >> 5;
    const unsigned lo = lane * 16;
    if (tid == 0) set_flag(ws, WS_F2 + qb * 256 + h2 * 8, epoch);
    if (!act3) {
        const int id = ((bi >> 4) * 2 + (bi & 1)) * 8 + xcd;
        const unsigned v2 = (lane >> 2) * 896 + (lane & 3) * 16, trp = (unsigned)((4 * hh + ((lane & 15) >> 2)) * 64 + ((lane >> 4) & 1) * 32 + (lane & 3) * 8);
        const unsigned so = id < 28 ? (unsigned)(((id & 3) * 28 + w) * 16 * 896 + (id >> 2) * 128) : OOB, ld = w * 8 * KB;
#pragma unroll
        for (int i = 0; i < 4; ++i) { dma(rW2, v2, so + i * 128 * 896, ld + 2 * i * KB); dma(rW2, v2, so + i * 128 * 896 + 64, ld + (2 * i + 1) * KB); }
        const unsigned dst = id < 28 ? WS_W2T + (unsigned)((id >> 2) * 224 + (id & 3) * 28 + w) * 1024 : OOB;
        waitv<0>();
#pragma unroll
        for (int i = 0; i < 8; ++i) st16(rWs, wfrag<512>(ld + trp + i * KB), lo, (i >= 6 && w >= 4) ? OOB : dst + ((i & 1) * 112 + (i >> 1) * 8) * 1024);
        waitv<0>();
        BAR();
        if (tid == 0) set_flag(ws, WS_FS + 256 + id * 8, epoch);
        if (b == 14 * 8 && w == 0) {
#pragma unroll 1
            for (int i = 0; i < 4; ++i) wait_flags(ws, WS_F2 + (unsigned)((i * 4 + (lane >> 4)) * 256 + (lane & 15) * 8), epoch);
            if (lane == 0) set_flag(ws, WS_EPOCH, epoch);
        }
        return;
    }

    const int mt = w & 1, kq = w >> 1;
    const unsigned tt0 = (unsigned)(tb3 * 4 + mh * 2), grp = (unsigned)(tb3 * 2 + mh);
    const unsigned xo = ((tt0 + mt) * 32 + r) * 896 + (cb * 64 + 16 * kq + 4 * hh) * 2;
    u32x4 fr[28];
    u32x4 hres;
    f32x4 cs[4];
    {
        STAMP(3); STAMP(27);
        const u32x2 xr0 = __builtin_amdgcn_raw_buffer_load_b64(rX, xo, 0, 0), xr1 = __builtin_amdgcn_raw_buffer_load_b64(rX, xo + 16, 0, 0);
#pragma unroll
        for (int g = 0; g < 4; ++g) cs[g] = __builtin_bit_cast(f32x4, ld16(rWs, (8 * g + 4 * hh) * 4, WS_CS1 + (unsigned)(cb * 256 + w * 32) * 4));
#pragma unroll
        for (int i = 0; i < NPK; ++i) fr[i] = fe[i];
#pragma unroll
        for (int i = NPK; i < NW1A; ++i) fr[i] = ld16(rWs, lo, w1b + R4(i) * 1024);
        if (w == 0) wait_flags(ws, WS_F2 + tb3 * 256 + (lane & 15) * 8, epoch);
        BARX();
        STAMP(4); STAMP(28);
        u32x4 bf[8];
#pragma unroll
        for (int i = 0; i < 8; ++i) bf[i] = ld16(rWs, lo + (i & 3) * 1024, WS_O + ((tt0 + mt) * 32 + 8 * kq + (i & 4)) * 1024);
        SB();
        f32x16 a0 = {}, a1 = {};
#pragma unroll
        for (int i = 0; i < 8; ++i) { a0 = MFMA(fa[2 * i], bf[i], a0); a1 = MFMA(fa[2 * i + 1], bf[i], a1); }
        PIN(a0); PIN(a1);
        SB();
#pragma unroll
        for (int i = NW1A; i < NW1B; ++i) fr[i] = ld16(rWs, lo, w1b + R4(i) * 1024);
        SB();
        STAMP(23);
        {
            const unsigned dst = L_RED + (unsigned)(mt * 16 + kq) * 2048 + lo;
#pragma unroll
            for (int p = 0; p < 4; ++p) {
                const f32x16& o = (p >> 1) ? a1 : a0;
                *(LAS f32x4*)(uintptr_t)(dst + p * 8192) = f32x4{o[8 * (p & 1)], o[8 * (p & 1) + 1], o[8 * (p & 1) + 2], o[8 * (p & 1) + 3]};
                *(LAS f32x4*)(uintptr_t)(dst + p * 8192 + 1024) = f32x4{o[8 * (p & 1) + 4], o[8 * (p & 1) + 5], o[8 * (p & 1) + 6], o[8 * (p & 1) + 7]};
            }
        }
        float o8[8] = {bflo(xr0[0]), bfhi(xr0[0]), bflo(xr0[1]), bfhi(xr0[1]), bflo(xr1[0]), bfhi(xr1[0]), bflo(xr1[1]), bfhi(xr1[1])};
        unsigned src = L_RED + (unsigned)(mt * 4 + kq) * 8192 + lo;
#pragma unroll
        for (int i = 0; i < 8; ++i) PIN(o8[i]);
        PIN(src);
        BAR();
        STAMP(24);
#pragma unroll
        for (int i = 0; i < 4; ++i) {
            const f32x4 a = lds16f(src + i * 2048), c = lds16f(src + i * 2048 + 1024);
#pragma unroll
            for (int e = 0; e < 4; ++e) { o8[e] += a[e]; o8[4 + e] += c[e]; }
        }
        hres = u32x4{pk(o8[0], o8[1]), pk(o8[2], o8[3]), pk(o8[4], o8[5]), pk(o8[6], o8[7])};
        st16(rWs, hres, lo, WS_H + ((tt0 + mt) * 28 + cb * 4 + kq) * 1024);
        float s1 = 0.f, s2 = 0.f;
#pragma unroll
        for (int e = 0; e < 4; ++e) { const float a = bflo(hres[e]), c = bfhi(hres[e]); s1 += a + c; s2 = fmaf(a, a, fmaf(c, c, s2)); }
        s1 = sum32(s1); s2 = sum32(s2);
        st8(rWs, u32x2{__float_as_uint(s1), __float_as_uint(s2)}, hh ? OOB : r * 8, WS_ST + ((tt0 + mt) * 28 + cb * 4 + kq) * 256);
        waitv<0>();
    }
    STAMP(5);
    float rstd2;
    u32x4 f2[28];
    const int r5 = 2 * ((cb + 2 * g4) % 7);
    const unsigned w2b = WS_W2T + (unsigned)(cb * 2 * 112 + 14 * w) * 1024;
#define R5(t) ((t) + r5 >= 14 ? (t) + r5 - 14 : (t) + r5)
    {
        BARX();
        if (tid == 0) set_flag(ws, WS_F3 + grp * 256 + cb * 8, epoch);
        SB();
#pragma unroll
        for (int i = NW1B; i < 28; ++i) fr[i] = ld16(rWs, lo, w1b + R4(i) * 1024);
#pragma unroll
        for (int i = 0; i < 28; ++i) PIN(fr[i]);
        u32x2 stv[7];
        if (w < 4) {
            if (w == 0) {
                wait_flags(ws, lane < 32 ? WS_F3 + grp * 256 + (unsigned)(lane % 7) * 8 : WS_FS + 256 + (unsigned)(cb * 4 + (lane & 3)) * 8, epoch); *(volatile LAS unsigned*)(uintptr_t)L_GO = 3u;
            } else { while (*(const volatile LAS unsigned*)(uintptr_t)L_GO != 3u) __builtin_amdgcn_s_sleep(1); asm volatile("" ::: "memory"); }
            STAMP(6);
#pragma unroll
            for (int i = 0; i < 14; ++i) dma_sc1(rWs, lo, WS_H + ((tt0 + i / 7) * 28 + R4(4 * (i % 7) + w)) * 1024, L_A + ((i / 7) * 28 + 4 * (i % 7) + w) * KB);
            waitv<7>();
        } else {
            while (*(const volatile LAS unsigned*)(uintptr_t)L_GO != 3u) __builtin_amdgcn_s_sleep(1);
            asm volatile("" ::: "memory");
            STAMP(6);
#pragma unroll
            for (int i = 0; i < 7; ++i) stv[i] = __builtin_amdgcn_raw_buffer_load_b64(rWs, (unsigned)(hh * 256 + r * 8), WS_ST + ((tt0 + (w & 1)) * 28 + 4 * i + (w & 2)) * 256, 16);
        }
        BARX();
        STAMP(17);
        f32x16 a0 = {}, a1 = {};
        u32x4 bq[4];
        bq[0] = lds16(L_A + lo); bq[1] = lds16(L_A + KB + lo); bq[2] = lds16(L_A + 2 * KB + lo);
        SB();
        f32x2 sv[2][4];
        float nm[2], ce[2], rs1;
        u32x4 o[2];
        const unsigned hso = WS_H1 + (tt0 * 112 + cb * 16 + w * 2) * 1024;
#define W2LD(q) f2[q] = ld16(rWs, lo, w2b + (unsigned)(((q) & 1) * 112 + R5((q) >> 1)) * 1024)
        auto step = [&](auto sc) __attribute__((always_inline)) {
            constexpr int s = decltype(sc)::value;
            if (s == 16) {
                if (w >= 4) {
                    float s1 = 0.f, s2 = 0.f;
#pragma unroll
                    for (int i = 0; i < 7; ++i) { s1 = add1(s1, __uint_as_float(stv[i][0])); s2 = add1(s2, __uint_as_float(stv[i][1])); }
                    *(LAS f32x2*)(uintptr_t)(L_M + (w - 4) * 512 + lane * 8) = f32x2{s1, s2};
                } else waitv<4>();
                BAR();
            }
            if (s + 3 < 56) bq[(s + 3) & 3] = lds16(L_A + (s + 3) * KB + lo);
            if constexpr (s == 17 || s == 18) {
#pragma unroll
                for (int i = 0; i < 4; ++i) sv[s - 17][i] = *(const LAS f32x2*)(uintptr_t)(L_M + ((i >> 1) * 2 + (s - 17)) * 512 + ((i & 1) * 32 + r) * 8);
            }
            SB();
            if constexpr (s < 28) { a0 = MFMA(fr[s % 28], bq[s & 3], a0); PIN(a0); } else { a1 = MFMA(fr[s % 28], bq[s & 3], a1); PIN(a1); }
            SB();
            if constexpr (s < 28 && s % 4 == 3) W2LD(s / 4);
            if constexpr (s >= 28 && s % 3 == 0 && 7 + (s - 28) / 3 < NW2) W2LD(7 + (s - 28) / 3);
            if constexpr (s == 22 || s == 24) {
                const int m = (s - 22) / 2;
                const float s1 = add1(add1(sv[m][0][0], sv[m][1][0]), add1(sv[m][2][0], sv[m][3][0])), s2 = add1(add1(sv[m][0][1], sv[m][1][1]), add1(sv[m][2][1], sv[m][3][1]));
                const float mean = s1 * (1.f / DM), rs = __builtin_amdgcn_rsqf(fmaxf(s2 * (1.f / DM) - mean * mean, 0.f) + 1e-5f);
                nm[m] = -mean; ce[m] = rs * -1.4426950408889634f;
                if (m == 0) rs1 = rs; else rs1 = mt ? rs : rs1;
                PIN(nm[m]); PIN(ce[m]); PIN(rs1);
            }
            if constexpr (s == 32 || s == 37 || s == 42 || s == 47) {
                const int g = (s - 32) / 5;
                const u32x2 y = silu4(a0[4 * g], a0[4 * g + 1], a0[4 * g + 2], a0[4 * g + 3], cs[g], nm[0], ce[0]);
                o[g >> 1][(g & 1) * 2] = y[0]; o[g >> 1][(g & 1) * 2 + 1] = y[1];
                PIN(o[g >> 1]);
                if (g & 1) st16(rWs, o[g >> 1], lo + (g >> 1) * 1024, hso);
            }
            SB();
        };
        static_for<0, 56>(step);
        rstd2 = rs1;
        asm volatile("s_nop 15" : "+v"(a1));
        STAMP(18);
#pragma unroll
        for (int g = 0; g < 4; ++g) {
            const u32x2 y = silu4(a1[4 * g], a1[4 * g + 1], a1[4 * g + 2], a1[4 * g + 3], cs[g], nm[1], ce[1]);
            o[g >> 1][(g & 1) * 2] = y[0]; o[g >> 1][(g & 1) * 2 + 1] = y[1];
            if (g & 1) st16(rWs, o[g >> 1], lo + (g >> 1) * 1024, hso + 112 * 1024);
        }
        STAMP(19);
        waitv<0>();
    }
    STAMP(7);
    {
        BAR();
        if (tid == 0) set_flag(ws, WS_F4 + grp * 256 + cb * 8, epoch);
        SB();
#pragma unroll
        for (int q = NW2; q < 28; ++q) f2[q] = ld16(rWs, lo, w2b + (unsigned)((q & 1) * 112 + R5(q >> 1)) * 1024);
        if (w == 0) wait_flags(ws, WS_F4 + grp * 256 + (unsigned)(lane % 7) * 8, epoch);
        BARX();
        STAMP(8);
        u32x4 bf[28];
        const unsigned bs = WS_H1 + (tt0 * 112 + 14 * w) * 1024;
#define BLD(t) do { const unsigned o_ = bs + R5(t) * 1024; bf[2 * (t)] = ld16(rWs, lo, o_); bf[2 * (t) + 1] = ld16(rWs, lo, o_ + 112 * 1024); } while (0)
#pragma unroll
        for (int t = 0; t < NB5; ++t) BLD(t);
        SB();
        f32x16 ac[4] = {{}, {}, {}, {}};
#pragma unroll
        for (int t = 0; t < 14; ++t) {
            ac[0] = MFMA(f2[2 * t], bf[2 * t], ac[0]); ac[2] = MFMA(f2[2 * t + 1], bf[2 * t], ac[2]);
            ac[1] = MFMA(f2[2 * t], bf[2 * t + 1], ac[1]); ac[3] = MFMA(f2[2 * t + 1], bf[2 * t + 1], ac[3]);
            PIN(ac[0]); PIN(ac[1]); PIN(ac[2]); PIN(ac[3]);
            SB();
            if (t + NB5 < 14) BLD(t + NB5);
            SB();
            if (t == 0) STAMP(20); if (t == 13) STAMP(21);
        }
#pragma unroll
        for (int p = 0; p < 8; ++p) {
            const f32x16& o = ac[(p >> 2) * 2 + (p & 1)]; const int h8 = ((p >> 1) & 1) * 8;
            const unsigned dst = (unsigned)(p * 8 + w) * 2048 + lo;
            *(LAS f32x4*)(uintptr_t)dst = f32x4{o[h8], o[h8 + 1], o[h8 + 2], o[h8 + 3]};
            *(LAS f32x4*)(uintptr_t)(dst + 1024) = f32x4{o[h8 + 4], o[h8 + 5], o[h8 + 6], o[h8 + 7]};
        }
        BAR();
        f32x4 y0 = {0.f, 0.f, 0.f, 0.f}, y1 = y0;
#pragma unroll
        for (int i = 0; i < 8; ++i) { const unsigned src = (unsigned)(w * 8 + i) * 2048 + lo; y0 += lds16f(src); y1 += lds16f(src + 1024); }
        const u32x2 z0 = {pk(fmaf(y0[0], rstd2, bflo(hres[0])), fmaf(y0[1], rstd2, bfhi(hres[0]))), pk(fmaf(y0[2], rstd2, bflo(hres[1])), fmaf(y0[3], rstd2, bfhi(hres[1])))};
        const u32x2 z1 = {pk(fmaf(y1[0], rstd2, bflo(hres[2])), fmaf(y1[1], rstd2, bfhi(hres[2]))), pk(fmaf(y1[2], rstd2, bflo(hres[3])), fmaf(y1[3], rstd2, bfhi(hres[3])))};
        __builtin_amdgcn_raw_buffer_store_b64(z0, rOut, xo, 0, 0);
        __builtin_amdgcn_raw_buffer_store_b64(z1, rOut, xo + 16, 0, 0);
    }
    STAMP(9); STAMP(30);
}

extern "C" void kernel_launch(void* const* d_in, const int* in_sizes, int n_in, void* d_out, int out_size, void* d_ws, size_t ws_size,
                              hipStream_t stream) {
    (void)hipFuncSetAttribute((const void*)block_kernel, hipFuncAttributeMaxDynamicSharedMemorySize, LDS_BYTES);
    block_kernel<<<256, 512, LDS_BYTES, stream>>>(d_in[0], d_in[1], d_in[2], d_in[4], d_in[5], d_out, (char*)d_ws);
}
```

```cpp
#include <hip/hip_runtime.h>
#include <stdint.h>

typedef __attribute__((ext_vector_type(8))) __bf16 bf16x8;
typedef __attribute__((ext_vector_type(2))) __bf16 bf16x2;
typedef __attribute__((ext_vector_type(4))) short s16x4;
typedef __attribute__((ext_vector_type(16))) float f32x16;
typedef __attribute__((ext_vector_type(4))) float f32x4;
typedef __attribute__((ext_vector_type(2))) float f32x2;
typedef __attribute__((ext_vector_type(4))) unsigned u32x4;
typedef __attribute__((ext_vector_type(2))) unsigned u32x2;
typedef unsigned long long u64;
#define LAS __attribute__((address_space(3)))
#define GAS __attribute__((address_space(1)))
#define DI __device__ __forceinline__
typedef __amdgpu_buffer_rsrc_t rsrc_t;

constexpr int SEQ = 2048, DM = 448, DH = 28, FF = 1792;
constexpr unsigned WS_EPOCH = 0;
constexpr unsigned WS_F1 = 4096;
constexpr unsigned WS_FS = WS_F1 + 16 * 256;
constexpr unsigned WS_F2 = WS_FS + 1024;
constexpr unsigned WS_F3 = WS_F2 + 16 * 256;
constexpr unsigned WS_F4 = WS_F3 + 32 * 256;
constexpr unsigned WS_CS1 = 65536;
constexpr unsigned WS_Q = 131072;
constexpr unsigned WS_K = WS_Q + 64 * 28 * 1024;
constexpr unsigned WS_V = WS_K + 64 * 28 * 1024;
constexpr unsigned WS_O = WS_V + 64 * 28 * 1024;
constexpr unsigned WS_H = WS_O + 64 * 32 * 1024;
constexpr unsigned WS_ST = WS_H + 64 * 28 * 1024;
constexpr unsigned WS_H1 = WS_ST + 64 * 28 * 256;
constexpr unsigned WS_W1T = WS_H1 + 64 * 112 * 1024;
constexpr unsigned WS_W2T = WS_W1T + 56 * 28 * 1024;
constexpr unsigned WS_END = WS_W2T + 14 * 112 * 1024;
constexpr unsigned KB = 1024;
constexpr unsigned L_XR = 0, L_WR = 64 * KB, L_WQ = 120 * KB, L_CS = 148 * KB, L_XS = L_CS + 1024;
constexpr unsigned L_A = 0, L_CMB = 64 * KB, L_RED = 64 * KB, L_M = 152 * KB;
constexpr unsigned L_BAD = 159 * KB, L_CNT = L_BAD + 4, L_GO = L_BAD + 8;
constexpr unsigned LDS_BYTES = 160 * KB;
constexpr unsigned SPIN_CAP = 1u << 16;
constexpr unsigned OOB = 0x7f000000u;

constexpr int NPK = 12, NW1A = 19, NW1B = 23, NW2 = 16, NB5 = 5, PD4 = 6, LTS = 1;

#define MFMA(a, b, c) __builtin_amdgcn_mfma_f32_32x32x16_bf16(__builtin_bit_cast(bf16x8, a), __builtin_bit_cast(bf16x8, b), c, 0, 0, 0)
#define BAR() asm volatile("s_waitcnt lgkmcnt(0)\n\ts_barrier" ::: "memory")
#define BARX() asm volatile("s_barrier" ::: "memory")
#define LGK() asm volatile("s_waitcnt lgkmcnt(0)" ::: "memory")
#define SB() __builtin_amdgcn_sched_barrier(0)
#define PIN(x) asm volatile("" : "+v"(x))
#define EX2(x) __builtin_amdgcn_exp2f(x)
#define RLX_AGENT __ATOMIC_RELAXED, __HIP_MEMORY_SCOPE_AGENT

template <int I> struct IC { static constexpr int value = I; };
template <int I, int E, class F> DI void static_for(F& f) { if constexpr (I < E) { f(IC<I>{}); static_for<I + 1, E>(f); } }
template <int N> DI void waitv() { asm volatile("s_waitcnt vmcnt(%0)" ::"n"(N) : "memory"); }
DI void dma(rsrc_t rs, unsigned voff, unsigned soff, unsigned lds) {
    asm volatile("s_mov_b32 m0, %0\n\ts_nop 0\n\tbuffer_load_dwordx4 %1, %2, %3 offen lds" ::"s"(lds), "v"(voff), "s"(rs), "s"(soff) : "memory");
}
DI void dma_sc1(rsrc_t rs, unsigned voff, unsigned soff, unsigned lds) {
    asm volatile("s_mov_b32 m0, %0\n\ts_nop 0\n\tbuffer_load_dwordx4 %1, %2, %3 offen sc1 lds" ::"s"(lds), "v"(voff), "s"(rs), "s"(soff) : "memory");
}
DI u32x4 ld16(rsrc_t rs, unsigned voff, unsigned soff) { return __builtin_amdgcn_raw_buffer_load_b128(rs, voff, soff, 16); }
DI void st16(rsrc_t rs, u32x4 v, unsigned voff, unsigned soff) { __builtin_amdgcn_raw_buffer_store_b128(v, rs, voff + soff, 0, 16); }
DI void st8(rsrc_t rs, u32x2 v, unsigned voff, unsigned soff) { __builtin_amdgcn_raw_buffer_store_b64(v, rs, voff, soff, 16); }
DI u32x4 lds16(unsigned a) { return *(const LAS u32x4*)(uintptr_t)a; }
DI f32x4 lds16f(unsigned a) { return *(const LAS f32x4*)(uintptr_t)a; }
DI u32x2 ldstr(unsigned a) { return __builtin_bit_cast(u32x2, __builtin_amdgcn_ds_read_tr16_b64_v4i16((LAS s16x4*)(uintptr_t)a)); }
template <int D2> DI u32x4 wfrag(unsigned a) { u32x2 lo = ldstr(a), hi = ldstr(a + D2); return u32x4{lo[0], lo[1], hi[0], hi[1]}; }
DI unsigned pk(float lo, float hi) { return __builtin_bit_cast(unsigned, __builtin_convertvector(f32x2{lo, hi}, bf16x2)); }
DI float bflo(unsigned u) { return __uint_as_float(u << 16); }
DI float bfhi(unsigned u) { return __uint_as_float(u & 0xffff0000u); }
DI float sum32(float v) {
    auto r = __builtin_amdgcn_permlane32_swap(__float_as_uint(v), __float_as_uint(v), false, false);
    return __uint_as_float(r[0]) + __uint_as_float(r[1]);
}
DI float max32(float v) {
    auto r = __builtin_amdgcn_permlane32_swap(__float_as_uint(v), __float_as_uint(v), false, false);
    return fmaxf(__uint_as_float(r[0]), __uint_as_float(r[1]));
}
DI int fresh_lane() { int l; asm volatile("v_mbcnt_lo_u32_b32 %0, -1, 0\n\tv_mbcnt_hi_u32_b32 %0, -1, %0" : "=v"(l)); return l; }
DI float add1(float a, float b) { float d; asm("v_add_f32 %0, %1, %2" : "=v"(d) : "v"(a), "v"(b)); return d; }
DI float mul1(float a, float b) { float d; asm("v_mul_f32 %0, %1, %2" : "=v"(d) : "v"(a), "v"(b)); return d; }
DI float fma1(float a, float b, float c) { float d; asm("v_fma_f32 %0, %1, %2, %3" : "=v"(d) : "v"(a), "v"(b), "v"(c)); return d; }
DI u32x2 silu4(float a0, float a1, float a2, float a3, f32x4 c, float nm, float ce) {
    unsigned o0, o1; float z0, z1, z2, z3, t0, t1, t2, t3;
    asm("v_fma_f32 %2, %14, %10, %16\n\tv_fma_f32 %3, %14, %11, %17\n\tv_fma_f32 %4, %14, %12, %18\n\tv_fma_f32 %5, %14, %13, %19\n\t"
        "v_mul_f32 %6, %15, %2\n\tv_mul_f32 %7, %15, %3\n\tv_mul_f32 %8, %15, %4\n\tv_mul_f32 %9, %15, %5\n\t"
        "v_exp_f32 %6, %6\n\tv_exp_f32 %7, %7\n\tv_exp_f32 %8, %8\n\tv_exp_f32 %9, %9\n\t"
        "v_add_f32 %6, 1.0, %6\n\tv_add_f32 %7, 1.0, %7\n\tv_add_f32 %8, 1.0, %8\n\tv_add_f32 %9, 1.0, %9\n\t"
        "v_rcp_f32 %6, %6\n\tv_rcp_f32 %7, %7\n\tv_rcp_f32 %8, %8\n\tv_rcp_f32 %9, %9\n\t"
        "v_mul_f32 %2, %2, %6\n\tv_mul_f32 %3, %3, %7\n\tv_mul_f32 %4, %4, %8\n\tv_mul_f32 %5, %5, %9\n\t"
        "v_cvt_pk_bf16_f32 %0, %2, %3\n\tv_cvt_pk_bf16_f32 %1, %4, %5"
        : "=v"(o0), "=v"(o1), "=&v"(z0), "=&v"(z1), "=&v"(z2), "=&v"(z3), "=&v"(t0), "=&v"(t1), "=&v"(t2), "=&v"(t3)
        : "v"(c[0]), "v"(c[1]), "v"(c[2]), "v"(c[3]), "v"(nm), "v"(ce), "v"(a0), "v"(a1), "v"(a2), "v"(a3));
    return u32x2{o0, o1};
}
DI float dot2(unsigned a, unsigned b, float c) { return __builtin_amdgcn_fdot2_f32_bf16(__builtin_bit_cast(bf16x2, a), __builtin_bit_cast(bf16x2, b), c, false); }

DI void wait_flags(char* ws, unsigned off, u64 epoch) {
    const GAS u64* p = (const GAS u64*)(ws + off);
    for (unsigned sp = 0; sp < SPIN_CAP; ++sp) {
        const u64 v = __hip_atomic_load(p, RLX_AGENT);
        if (__all(v == epoch)) break;
        __builtin_amdgcn_s_sleep(4);
    }
    asm volatile("" ::: "memory");
}
DI void wait_flags2(char* ws, unsigned off0, unsigned off1, u64 epoch) {
    const GAS u64* p0 = (const GAS u64*)(ws + off0); const GAS u64* p1 = (const GAS u64*)(ws + off1);
    for (unsigned sp = 0; sp < SPIN_CAP; ++sp) {
        const u64 v0 = __hip_atomic_load(p0, RLX_AGENT), v1 = __hip_atomic_load(p1, RLX_AGENT);
        if (__all(v0 == epoch && v1 == epoch)) break;
        __builtin_amdgcn_s_sleep(8);
    }
    asm volatile("" ::: "memory");
}
DI void set_flag(char* ws, unsigned off, u64 epoch) { __hip_atomic_store((GAS u64*)(ws + off), epoch, RLX_AGENT); }

__global__ void __launch_bounds__(512, 2)
block_kernel(const void* __restrict__ gx, const void* __restrict__ gwqkv, const void* __restrict__ gwo, const void* __restrict__ gw1,
             const void* __restrict__ gw2, void* __restrict__ gout, char* __restrict__ ws) {
    extern __shared__ __attribute__((aligned(16))) char smem[];
    const int tid = threadIdx.x;
    const int w = __builtin_amdgcn_readfirstlane(tid >> 6);
    const int b = blockIdx.x, xcd = b & 7, bi = b >> 3;
    {
        u64 pc; unsigned clen;
        asm volatile("s_getpc_b64 %0\n.Lblk_code_begin:\n\ts_mov_b32 %1, .Lblk_code_end-.Lblk_code_begin" : "=s"(pc), "=s"(clen));
        clen = clen < 65536u ? clen : 0u;
        dma(__builtin_amdgcn_make_buffer_rsrc((void*)pc, 0, clen, 0x00020000), (tid & 63) < 2 ? (tid & 63) * 32768 : OOB, (unsigned)(bi * 8 + w) * 128, L_M + 4096);
    }
    const rsrc_t rX = __builtin_amdgcn_make_buffer_rsrc((void*)gx, 0, SEQ * DM * 2, 0x00020000);
    const rsrc_t rWqkv = __builtin_amdgcn_make_buffer_rsrc((void*)gwqkv, 0, DM * 3 * DM * 2, 0x00020000);
    const rsrc_t rW2 = __builtin_amdgcn_make_buffer_rsrc((void*)gw2, 0, FF * DM * 2, 0x00020000);
    const rsrc_t rOut = __builtin_amdgcn_make_buffer_rsrc(gout, 0, SEQ * DM * 2, 0x00020000);
    const rsrc_t rWs = __builtin_amdgcn_make_buffer_rsrc(ws, 0, WS_END, 0x00020000);
    const int tb1 = 4 * (xcd >> 1) + bi / 7, c1 = 7 * (xcd & 1) + bi % 7;
    const int h2 = 2 * xcd + (bi & 1), qb = bi >> 1;
    const int tb3 = 2 * xcd + (bi >> 4), j3 = bi & 15, mh = j3 & 1, cb = j3 >> 1;
    const bool act3 = j3 < 14;
    const int g4 = (bi >> 4) * 2 + mh, r4 = 7 * g4;
#define R4(s) ((s) + r4 >= 28 ? (s) + r4 - 28 : (s) + r4)
    u32x4 fa[16];
    u32x4 fe[NPK];
    const unsigned w1b = act3 ? WS_W1T + (unsigned)(cb * 8 + w) * 28 * 1024 : OOB;
    u64 epoch;
#define EPOCH() do { asm volatile("s_load_dwordx2 %0, %1, 0x0\n\ts_waitcnt lgkmcnt(0)" : "=s"(epoch) : "s"(ws + WS_EPOCH) : "memory"); epoch += 1; } while (0)
    {
    const int lane = tid & 63, r = lane & 31, hh = lane >> 5;
    const unsigned trn = (unsigned)((8 * hh + ((lane & 15) >> 2)) * 64 + ((lane >> 4) & 1) * 32 + (lane & 3) * 8);
    const unsigned trp = (unsigned)((4 * hh + ((lane & 15) >> 2)) * 64 + ((lane >> 4) & 1) * 32 + (lane & 3) * 8);
    const unsigned v2 = (lane >> 2) * 896 + (lane & 3) * 16;
    const unsigned lo = lane * 16;
    if (tid == 0) *(LAS u32x4*)(uintptr_t)L_BAD = u32x4{0u, 0u, 0u, 0u};

#define EARLY_TOUCH(j) do { \
        if ((j) == 0) dma(rX, lane < 56 ? lane * 128 : OOB, (unsigned)b * 7168, L_M + 4096); \
        else if ((j) == 1) dma(rWqkv, lane < 37 ? lane * 128 : OOB, (unsigned)b * 4704, L_M + 4096); \
        else dma(rX, OOB, 0, L_M + 4096); } while (0)
#define LATE_TOUCH(j) do { \
        if ((j) == 0) dma_sc1(rWs, lane < 16 ? lane * 2097152 : lane == 16 ? WS_F1 + c1 * 256 : lane == 17 ? WS_F2 + qb * 256 : lane == 18 ? WS_F3 + (tb3 * 2 + mh) * 256 : lane == 19 ? WS_F4 + (tb3 * 2 + mh) * 256 : lane == 20 ? WS_F1 + c1 * 256 + 128 : OOB, 0, L_M + 4096); \
        else if ((j) == 1) dma_sc1(rOut, lane < 7 ? lane * 262144 : OOB, 0, L_M + 4096); \
        else dma(rOut, OOB, 0, L_M + 4096); } while (0)
    {
        const bool sp = bi >= 28, xw = !sp && w < 4;
        const int rin = lane >> 3, sl = lane & 7, id = (bi - 28) * 8 + xcd;
        const rsrc_t rs = __builtin_amdgcn_make_buffer_rsrc((void*)(sp ? gw1 : xw ? gx : gwqkv), 0, sp ? DM * FF * 2 : xw ? SEQ * DM * 2 : DM * 3 * DM * 2, 0x00020000);
        const unsigned vw = (lane >> 2) * (sp ? 3584 : 2688) + (lane & 3) * 16;
        const unsigned ve = xw ? rin * 896 + ((sl ^ (rin >> 1)) * 16) : vw, vo = xw ? rin * 896 + ((sl ^ (4 + (rin >> 1))) * 16) : vw;
        const unsigned so = sp ? (id < 28 ? (unsigned)(w * 16 * 3584 + id * 128) : OOB) : xw ? (unsigned)(tb1 * 128 + w * 32) * 896 : (unsigned)((w - 4) * 16 * 2688 + 896 + c1 * 128);
        const unsigned s0 = xw ? 7168 : 64, s1 = sp ? 128 * 3584 : xw ? 14336 : 64 * 2688, s2 = xw ? 128 : 2 * s1;
        const unsigned ld = sp ? w * 8 * KB : xw ? L_XR + w * 16 * KB : L_WR + (w - 4) * 2 * KB, l1 = (sp || xw) ? 2 * KB : 8 * KB;
#pragma unroll
        for (int i = 0; i < 8; ++i) dma(rs, (i & 1) ? vo : ve, so + (i & 1) * s0 + ((i >> 1) & 1) * s1 + (i >> 2) * s2, ld + (i & 1) * KB + (i >> 1) * l1);
        if (w < 4) EARLY_TOUCH(w);
        EPOCH();
    }
    if (bi >= 28) {
        const int id = (bi - 28) * 8 + xcd;
        if (w == 4 || w == 5) LATE_TOUCH(w - 4);
        if (w == 6) dma(rW2, lane < 7 ? lane * 262144 : OOB, 0, L_M + 4096);
        if (w == 7) dma(__builtin_amdgcn_make_buffer_rsrc((void*)gwo, 0, DM * DM * 2, 0x00020000), lane < 2 ? lane * 262144 : OOB, 0, L_M + 4096);
        const unsigned ld = w * 8 * KB;
        const unsigned dst = id < 28 ? WS_W1T + (unsigned)(id * 56 + w) * 1024 : OOB;
        waitv<0>();
        float c[2] = {0.f, 0.f};
#pragma unroll
        for (int i = 0; i < 8; ++i) {
            const u32x4 f = wfrag<512>(ld + trp + i * KB);
            st16(rWs, f, lo, (i >= 6 && w >= 4) ? OOB : dst + ((i & 1) * 28 + (i >> 1) * 8) * 1024);
#pragma unroll
            for (int e = 0; e < 4; ++e) c[i & 1] += bflo(f[e]) + bfhi(f[e]);
        }
        c[0] = sum32(c[0]); c[1] = sum32(c[1]);
        *(LAS float*)(uintptr_t)(L_M + w * 256 + lane * 4) = hh ? c[1] : c[0];
        BAR();
        if (w == 0) {
            float t = 0.f;
#pragma unroll
            for (int i = 0; i < 8; ++i) t += *(const LAS float*)(uintptr_t)(L_M + i * 256 + lane * 4);
            __builtin_amdgcn_raw_buffer_store_b32(__float_as_uint(t), rWs, lane * 4, id < 28 ? WS_CS1 + (unsigned)id * 256 : OOB, 16);
        }
        waitv<0>();
        BAR();
        if (tid == 0) set_flag(ws, WS_FS + id * 8, epoch);
        dma(__builtin_amdgcn_make_buffer_rsrc((void*)gwo, 0, DM * DM * 2, 0x00020000), lane < 13 ? lane * 128 : OOB, (unsigned)(id * 8 + w) * 1568, L_M + 4096);
        dma(rW2, lane < 49 ? lane * 128 : OOB, (unsigned)(id * 8 + w) * 6272, L_M + 4096);
    } else {
      const int mt1 = w & 3;
      const unsigned xl = L_XR + mt1 * 16 * KB, kt = (unsigned)(tb1 * 4 + mt1);
      const unsigned xb = xl + (r >> 3) * KB + (r & 7) * 128 + ((hh ^ ((r >> 1) & 7)) * 16);
      if (w < 4) {
        const int rin = lane >> 3, sl = lane & 7;
        const unsigned ve = rin * 896 + ((sl ^ (rin >> 1)) * 16), vo = rin * 896 + ((sl ^ (4 + (rin >> 1))) * 16);
        const unsigned xs = (unsigned)(tb1 * 128 + w * 32) * 896;
#define XDMA(c) do { _Pragma("unroll") for (int g = 0; g < 4; ++g) dma(rX, (g & 1) ? vo : ve, xs + (c) * 128 + g * 8 * 896, xl + ((c) & 3) * 4 * KB + g * KB); } while (0)
        u32x4 xf[8];
#define XRD(c) do { _Pragma("unroll") for (int s = 0; s < 4; ++s) xf[4 * ((c) & 1) + s] = lds16((xb ^ (s * 32)) + ((c) & 3) * 4 * KB); } while (0)
        f32x16 ac[2] = {{}, {}};
        unsigned wbA = L_WR + trn;
        PIN(wbA);
        u32x4 fw[3][2];
#define WRD(ks) do { fw[(ks) % 3][0] = wfrag<256>(wbA + (ks) * 2 * KB); fw[(ks) % 3][1] = wfrag<256>(wbA + (ks) * 2 * KB + KB); } while (0)
        float s1 = 0.f, s2 = 0.f;
#define XST(c) do { _Pragma("unroll") for (int q = 0; q < 4; ++q) _Pragma("unroll") for (int e = 0; e < 4; ++e) { s1 = dot2(xf[4 * ((c) & 1) + q][e], 0x3F803F80u, s1); s2 = dot2(xf[4 * ((c) & 1) + q][e], xf[4 * ((c) & 1) + q][e], s2); } PIN(s1); PIN(s2); SB(); } while (0)
        waitv<5>(); XRD(0); XDMA(2); XST(0);
        BARX(); WRD(0); WRD(1); SB();
#pragma unroll
        for (int ks = 0; ks < 28; ++ks) {
            ac[0] = MFMA(fw[ks % 3][0], xf[ks & 7], ac[0]); PIN(ac[0]);
            SB();
            if (ks + 2 < 28) {
                if ((ks + 2) % 4 == 0) {
                    const int c = (ks + 2) / 4;
                    if (c == 1) waitv<5>(); else if (c < 6) waitv<4>(); else waitv<0>();
                    XRD(c);
                    if (c + 2 < 7) XDMA(c + 2);
                    BARX();
                }
                WRD(ks + 2);
                if ((ks + 2) % 4 == 1 && ks > 2) XST((ks + 2) / 4);
            }
            SB();
            ac[1] = MFMA(fw[ks % 3][1], xf[ks & 7], ac[1]); PIN(ac[1]);
            SB();
        }
        s1 = sum32(s1); s2 = sum32(s2);
        const float mean = s1 * (1.f / DM), rstd = __builtin_amdgcn_rsqf(fmaxf(s2 * (1.f / DM) - mean * mean, 0.f) + 1e-5f), ms = -mean * rstd;
        if (hh == 0) *(LAS f32x2*)(uintptr_t)(L_XS + (w * 32 + r) * 8) = f32x2{mean, rstd};
        BAR();
        f32x4 csr[2][4];
#pragma unroll
        for (int t = 0; t < 2; ++t)
#pragma unroll
            for (int g = 0; g < 4; ++g) csr[t][g] = lds16f(L_CS + t * 128 + (8 * g + 4 * hh) * 4);
        u32x2 pkd[2][4];
#pragma unroll
        for (int t = 0; t < 2; ++t)
#pragma unroll
            for (int g = 0; g < 4; ++g) {
                float y[4];
#pragma unroll
                for (int e = 0; e < 4; ++e) y[e] = fma1(ac[t][4 * g + e], rstd, mul1(ms, csr[t][g][e]));
                pkd[t][g] = u32x2{pk(y[0], y[1]), pk(y[2], y[3])};
            }
        if (c1 < 7) {
            const unsigned base = WS_K + kt * 28672 + (unsigned)c1 * 4096;
#pragma unroll
            for (int q = 0; q < 4; ++q) {
                const int t = q >> 1, g = (q & 1) * 2;
                const auto x0 = __builtin_amdgcn_permlane32_swap(pkd[t][g][0], pkd[t][g + 1][0], false, false), x1 = __builtin_amdgcn_permlane32_swap(pkd[t][g][1], pkd[t][g + 1][1], false, false);
                st16(rWs, u32x4{x0[0], x1[0], x0[1], x1[1]}, lo + q * 1024, base);
            }
        } else {
            const unsigned vt = L_WR + w * 4 * KB;
#pragma unroll
            for (int t = 0; t < 2; ++t)
#pragma unroll
                for (int g = 0; g < 4; ++g) *(LAS u32x2*)(uintptr_t)(vt + t * 2 * KB + r * 64 + (2 * g + hh) * 8) = pkd[t][g];
            asm volatile("" ::: "memory");
            const unsigned off = (unsigned)(hh * 7168 + ((c1 - 7) * 64 + r) * 16);
#pragma unroll
            for (int t = 0; t < 2; ++t) {
                const u32x4 f0 = wfrag<512>(vt + t * 2 * KB + trp), f1 = wfrag<512>(vt + t * 2 * KB + trp + 1024);
                st16(rWs, f0, off + t * 512, WS_V + kt * 28672); st16(rWs, f1, off + t * 512 + 14336, WS_V + kt * 28672);
            }
        }
        BARX();
        waitv<0>();
        if (lane == 0 && __hip_atomic_fetch_add((LAS unsigned*)(uintptr_t)L_CNT, 1u, __ATOMIC_RELAXED, __HIP_MEMORY_SCOPE_WORKGROUP) == 3u) set_flag(ws, WS_F1 + c1 * 256 + tb1 * 8, epoch);
      } else {
        const int j = w - 4;
        const unsigned vw = (lane >> 2) * 2688 + (lane & 3) * 16;
        const unsigned so = (unsigned)(j * 16 * 2688 + 896 + c1 * 128), ld = L_WR + j * 2 * KB;
#define WDMA(st) do { dma(rWqkv, vw, so + (st) * 64 * 2688, ld + (st) * 8 * KB); dma(rWqkv, vw, so + (st) * 64 * 2688 + 64, ld + (st) * 8 * KB + KB); } while (0)
#define QDMA(st) dma(rWqkv, vw, (unsigned)((4 * (st) + j) * 16 * 2688 + c1 * 64), L_WQ + (4 * (st) + j) * KB)
        const u32x4 ones = {0x3F803F80u, 0x3F803F80u, 0x3F803F80u, 0x3F803F80u};
        f32x16 cacc = {}, aq = {};
        u32x4 xq[28];
        unsigned wb = L_WR + trn + (j & 1) * KB;
        PIN(wb);
#pragma unroll
        for (int st = 0; st < 7; ++st) {
            if (st < 2) waitv<6>(); else if (st < 4) waitv<7>(); else if (st == 4) waitv<8>(); else if (st == 5) waitv<10>(); else waitv<7>();
            if (st < 3) WDMA(st + 4);
            if (st == 3) { QDMA(0); QDMA(1); QDMA(2); }
            if (st == 4) { QDMA(3); QDMA(4); QDMA(5); QDMA(6); }
            if (st == LTS) LATE_TOUCH(j);
            BAR();
#pragma unroll
            for (int q = 0; q < 4; ++q) xq[4 * st + q] = lds16((xb ^ (q * 32)) + (st & 3) * 4 * KB);
            if (j < 2) {
                u32x4 f[4];
#pragma unroll
                for (int q = 0; q < 4; ++q) f[q] = wfrag<256>(wb + (4 * st + q) * 2 * KB);
#pragma unroll
                for (int q = 0; q < 4; ++q) cacc = MFMA(ones, f[q], cacc);
                PIN(cacc);
            }
            SB();
        }
        if (j < 2 && hh == 0) *(LAS float*)(uintptr_t)(L_CS + j * 128 + r * 4) = cacc[0];
        waitv<0>();
        BAR();
        unsigned qbs = L_WQ + trn;
        PIN(qbs);
        u32x4 fq[4];
        fq[0] = wfrag<256>(qbs); fq[1] = wfrag<256>(qbs + KB); fq[2] = wfrag<256>(qbs + 2 * KB);
        const f32x2 xs2 = *(const LAS f32x2*)(uintptr_t)(L_XS + (j * 32 + r) * 8);
        {
            cacc = f32x16{};
            u32x4 f[7];
#pragma unroll
            for (int i = 0; i < 7; ++i) f[i] = wfrag<256>(qbs + j * 7 * KB + i * KB);
#pragma unroll
            for (int i = 0; i < 7; ++i) cacc = MFMA(ones, f[i], cacc);
            PIN(cacc);
        }
        SB();
#pragma unroll
        for (int ks = 0; ks < 28; ++ks) {
            if (ks + 3 < 28) fq[(ks + 3) & 3] = wfrag<256>(qbs + (ks + 3) * KB);
            SB();
            aq = MFMA(fq[ks & 3], xq[ks], aq); PIN(aq);
            if (ks == 4 && hh == 0) *(LAS float*)(uintptr_t)(L_CS + 256 + j * 128 + r * 4) = cacc[0];
            SB();
        }
        BAR();
        const float sc = xs2[1] * (0.18898223650461362f * 1.4426950408889634f), ms = -xs2[0] * sc;
        u32x2 pkd[4];
#pragma unroll
        for (int g = 0; g < 4; ++g) {
            const unsigned ca_ = L_CS + 256 + (8 * g + 4 * hh) * 4;
            const f32x4 c = (lds16f(ca_) + lds16f(ca_ + 128)) + (lds16f(ca_ + 256) + lds16f(ca_ + 384));
            pkd[g] = u32x2{pk(fmaf(aq[4 * g], sc, ms * c[0]), fmaf(aq[4 * g + 1], sc, ms * c[1])), pk(fmaf(aq[4 * g + 2], sc, ms * c[2]), fmaf(aq[4 * g + 3], sc, ms * c[3]))};
        }
#pragma unroll
        for (int g = 0; g < 4; g += 2) {
            const auto x0 = __builtin_amdgcn_permlane32_swap(pkd[g][0], pkd[g + 1][0], false, false), x1 = __builtin_amdgcn_permlane32_swap(pkd[g][1], pkd[g + 1][1], false, false);
            st16(rWs, u32x4{x0[0], x1[0], x0[1], x1[1]}, lo + g * 512, WS_Q + kt * 28672 + (unsigned)c1 * 2048);
        }
        waitv<0>();
        if (lane == 0 && __hip_atomic_fetch_add((LAS unsigned*)(uintptr_t)(L_CNT + 8), 1u, __ATOMIC_RELAXED, __HIP_MEMORY_SCOPE_WORKGROUP) == 3u) set_flag(ws, WS_F1 + c1 * 256 + 128 + tb1 * 8, epoch);
      }
    }

    {
        const int qh = w & 1, kq = w >> 1;
        __builtin_amdgcn_s_setprio(3);
        const unsigned ch = (unsigned)((28 * h2) >> 3) * 512;
        const unsigned qso = WS_Q + (unsigned)(qb * 4 + qh * 2) * 28672 + ch;
        const rsrc_t rK = __builtin_amdgcn_make_buffer_rsrc(ws + WS_K + (unsigned)kq * 16 * 28672 + ch, 0, 15 * 28672 + 2048, 0x00020000);
        const rsrc_t rV = __builtin_amdgcn_make_buffer_rsrc(ws + WS_V + (unsigned)kq * 16 * 28672 + (unsigned)h2 * 448, 0, 16 * 28672 - h2 * 448, 0x00020000);
        unsigned vo0 = (unsigned)(hh * 7168 + r * 16), vo1 = vo0 + 14336;
        const unsigned vpad = r == DH ? 0x3F803F80u : 0u;
        unsigned qm0 = ((h2 & 1) && !hh) ? 0u : ~0u, qm1 = (!(h2 & 1) && hh) ? 0u : ~0u;
        u32x4 kf[4][2], vf[4][2], pa[2], pb[2], qa0, qa1, qb0, qb1;
        f32x16 oa = {}, ob = {}, sa, sb = {}, ca, cbv;
#pragma unroll
        for (int i = 0; i < 4; ++i) { vf[i][0] = vf[i][1] = u32x4{vpad, vpad, vpad, vpad}; PIN(vf[i][0]); PIN(vf[i][1]); }
        pb[0] = pb[1] = u32x4{0u, 0u, 0u, 0u};
#pragma unroll
        for (int i = 0; i < NPK; ++i) { fe[i] = pb[0]; PIN(fe[i]); }
        PIN(oa); PIN(ob); PIN(sb); PIN(pb[0]); PIN(pb[1]); PIN(vo0); PIN(vo1); PIN(qm0); PIN(qm1);
        SB();
        if (w == 0) {
            const int c0 = (28 * h2) & ~7;
            const int col = lane < 32 ? c0 + 31 * ((lane >> 4) & 1) : 448 + 28 * h2 + 27 * ((lane >> 4) & 1);
            wait_flags2(ws, WS_F1 + (unsigned)(col >> 6) * 256 + (lane & 15) * 8, lane < 32 ? WS_F1 + (unsigned)((c0 + 31 * (lane & 1)) >> 5) * 256 + 128 + qb * 8 : WS_FS + (unsigned)r * 8, epoch);
            *(volatile LAS unsigned*)(uintptr_t)L_GO = 1u;
        } else {
            if (w >= 4 && act3) {
                const rsrc_t rWo = __builtin_amdgcn_make_buffer_rsrc((void*)gwo, 0, DM * DM * 2, 0x00020000);
                __builtin_amdgcn_s_sleep(16);
#pragma unroll
                for (int i = 0; i < 8; ++i) { const int id = 4 * i + w - 4; const unsigned so = (unsigned)(((id >> 1) * 28 + (id & 1) * 16) * 896 + cb * 128); dma(rWo, v2, so, L_A + id * 2 * KB); dma(rWo, v2, so + 64, L_A + id * 2 * KB + KB); }
            }
            while (*(const volatile LAS unsigned*)(uintptr_t)L_GO == 0u) __builtin_amdgcn_s_sleep(1);
            asm volatile("" ::: "memory");
        }
        asm volatile("buffer_load_dwordx4 %0, %6, %7, %8 offen sc1\n\tbuffer_load_dwordx4 %1, %6, %7, %8 offen offset:1024 sc1\n\t"
                     "buffer_load_dwordx4 %4, %6, %7, %10 offen sc1\n\tbuffer_load_dwordx4 %5, %6, %7, %10 offen offset:1024 sc1\n\t"
                     "buffer_load_dwordx4 %2, %6, %7, %9 offen sc1\n\tbuffer_load_dwordx4 %3, %6, %7, %9 offen offset:1024 sc1"
                     : "=&v"(qa0), "=&v"(qa1), "=&v"(qb0), "=&v"(qb1), "=&v"(pa[0]), "=&v"(pa[1]) : "v"(lo), "s"(rWs), "s"(qso), "s"(qso + 28672), "s"(WS_K + ch) : "memory");
#define KVLD(KC, K0, K1, V0, V1, SK, SV) asm volatile("buffer_load_dwordx4 %0, %4, %7, %9 offen sc1\n\tbuffer_load_dwordx4 %1, %4, %7, %9 offen offset:1024 sc1\n\t" \
                "s_mov_b64 exec, %11\n\tbuffer_load_dwordx4 %2, %5, %8, %10 offen sc1\n\tbuffer_load_dwordx4 %3, %6, %8, %10 offen sc1\n\ts_mov_b64 exec, -1" \
                : KC(K0), KC(K1), "+v"(V0), "+v"(V1) : "v"(lo), "v"(vo0), "v"(vo1), "s"(rK), "s"(rV), "s"(SK), "s"(SV), "s"(0x0fffffff0fffffffull) : "memory")
#pragma unroll
        for (int i = 0; i < 4; ++i) KVLD("=&v", kf[i][0], kf[i][1], vf[(i + 3) & 3][0], vf[(i + 3) & 3][1], (unsigned)(i * 28672), i ? (unsigned)((i - 1) * 28672) : OOB);
        asm volatile("s_waitcnt vmcnt(16)" : "+v"(qa0), "+v"(qa1), "+v"(qb0), "+v"(qb1), "+v"(pa[0]), "+v"(pa[1]) :: "memory");
        qa0[0] &= qm0; qa0[1] &= qm0; qb0[0] &= qm0; qb0[1] &= qm0; qa1[2] &= qm1; qa1[3] &= qm1; qb1[2] &= qm1; qb1[3] &= qm1;
        {
            const f32x16 z = {};
            ca = MFMA(pa[0], qa0, z); cbv = MFMA(pa[0], qb0, z); ca = MFMA(pa[1], qa1, ca); cbv = MFMA(pa[1], qb1, cbv);
            float ma = ca[0], mb = cbv[0];
#pragma unroll
            for (int i = 1; i < 16; ++i) { ma = fmaxf(ma, ca[i]); mb = fmaxf(mb, cbv[i]); }
            ma = -max32(ma); mb = -max32(mb);
#pragma unroll
            for (int i = 0; i < 16; ++i) { ca[i] = ma; cbv[i] = mb; }
        }
        PIN(ca); PIN(cbv);
        asm volatile("s_waitcnt vmcnt(12)" : "+v"(kf[0][0]), "+v"(kf[0][1]), "+v"(vf[3][0]), "+v"(vf[3][1]) :: "memory");
        sa = MFMA(kf[0][0], qa0, ca); sa = MFMA(kf[0][1], qa1, sa);
        PIN(sa);
        SB();
#define GRP(MF, S, i, T, j, P) do { MF; SB(); \
        S[4 * (i)] = EX2(S[4 * (i)]); S[4 * (i) + 1] = EX2(S[4 * (i) + 1]); S[4 * (i) + 2] = EX2(S[4 * (i) + 2]); S[4 * (i) + 3] = EX2(S[4 * (i) + 3]); SB(); \
        P[(j) >> 1][((j) & 1) * 2] = pk(T[4 * (j)], T[4 * (j) + 1]); P[(j) >> 1][((j) & 1) * 2 + 1] = pk(T[4 * (j) + 2], T[4 * (j) + 3]); PIN(P[(j) >> 1]); PIN(S); SB(); } while (0)
#pragma unroll 1
        for (int tq = 0; tq < 4; ++tq) {
            const unsigned sk = (unsigned)tq * 4 * 28672;
#define FELD(i) asm volatile("buffer_load_dwordx4 %0, %1, %2, %3 offen sc1" : "+v"(fe[i]) : "v"(lo), "s"(rWs), "s"(w1b + R4(i) * 1024) : "memory")
            if (tq == 1) { FELD(0); FELD(1); FELD(2); FELD(3); }
            if (tq == 2) { FELD(4); FELD(5); FELD(6); FELD(7); }
            if (tq == 3) { FELD(8); FELD(9); FELD(10); FELD(11); }
            if (tq == 1) __builtin_amdgcn_s_setprio(2); if (tq == 2) __builtin_amdgcn_s_setprio(1); if (tq == 3) __builtin_amdgcn_s_setprio(0);
#pragma unroll
            for (int u = 0; u < 4; ++u) {
                GRP(ob = MFMA(vf[(u + 3) & 3][0], pb[0], ob), sa, 0, sb, 3, pb);
                GRP(sb = MFMA(kf[u][0], qb0, cbv), sa, 1, sa, 0, pa);
                GRP(sb = MFMA(kf[u][1], qb1, sb), sa, 2, sa, 1, pa);
                GRP(ob = MFMA(vf[(u + 3) & 3][1], pb[1], ob), sa, 3, sa, 2, pa);
                KVLD("+v", kf[u][0], kf[u][1], vf[(u + 3) & 3][0], vf[(u + 3) & 3][1], sk + (u + 4) * 28672, sk + (u + 3) * 28672);
                asm volatile("s_waitcnt vmcnt(12)" : "+v"(kf[(u + 1) & 3][0]), "+v"(kf[(u + 1) & 3][1]), "+v"(vf[u][0]), "+v"(vf[u][1]) :: "memory");
                SB();
                GRP(oa = MFMA(vf[u][0], pa[0], oa), sb, 0, sa, 3, pa);
                GRP(sa = MFMA(kf[(u + 1) & 3][0], qa0, ca), sb, 1, sb, 0, pb);
                GRP(sa = MFMA(kf[(u + 1) & 3][1], qa1, sa), sb, 2, sb, 1, pb);
                GRP(oa = MFMA(vf[u][1], pa[1], oa), sb, 3, sb, 2, pb);
            }
        }
        asm volatile("s_waitcnt vmcnt(0)" : "+v"(kf[0][0]), "+v"(kf[0][1]), "+v"(kf[1][0]), "+v"(kf[1][1]), "+v"(kf[2][0]), "+v"(kf[2][1]), "+v"(kf[3][0]), "+v"(kf[3][1]),
                     "+v"(vf[0][0]), "+v"(vf[0][1]), "+v"(vf[1][0]), "+v"(vf[1][1]), "+v"(vf[2][0]), "+v"(vf[2][1]) :: "memory");
#pragma unroll
        for (int i = 0; i < NPK; ++i) PIN(fe[i]);
        pb[1][2] = pk(sb[12], sb[13]); pb[1][3] = pk(sb[14], sb[15]);
        ob = MFMA(vf[3][0], pb[0], ob); ob = MFMA(vf[3][1], pb[1], ob);
        PIN(ob);
        const int lane = fresh_lane(), r = lane & 31, hh = lane >> 5;
        const unsigned lo = lane * 16;
        unsigned cw = L_CMB + (unsigned)(qh * 16 + kq) * 2048 + lo;
        PIN(cw);
#pragma unroll 1
        for (int pass = 0; pass < 2; ++pass) {
#pragma unroll
            for (int p = 0; p < 4; ++p) {
                const f32x16& o = (p >> 1) ? ob : oa;
                *(LAS f32x4*)(uintptr_t)(cw + p * 8192) = f32x4{o[8 * (p & 1)], o[8 * (p & 1) + 1], o[8 * (p & 1) + 2], o[8 * (p & 1) + 3]};
                *(LAS f32x4*)(uintptr_t)(cw + p * 8192 + 1024) = f32x4{o[8 * (p & 1) + 4], o[8 * (p & 1) + 5], o[8 * (p & 1) + 6], o[8 * (p & 1) + 7]};
            }
            unsigned so0 = WS_O + (unsigned)(((qb * 4 + qh * 2 + (kq >> 1)) * 16 + h2) * 2 + (kq & 1)) * 1024 + lo;
            unsigned cr = L_CMB + (unsigned)(qh * 4 + kq) * 8192 + lo, cl = L_CMB + (unsigned)(qh * 4 + (kq | 1)) * 8192 + 1024 + (32 + r) * 16;
            PIN(so0); PIN(cr); PIN(cl);
            BAR();
            f32x4 y0 = lds16f(cr), y1 = lds16f(cr + 1024);
            float l = *(const LAS float*)(uintptr_t)cl;
#pragma unroll
            for (int i = 1; i < 4; ++i) { y0 += lds16f(cr + i * 2048); y1 += lds16f(cr + i * 2048 + 1024); l += *(const LAS float*)(uintptr_t)(cl + i * 2048); }
            const float il = __builtin_amdgcn_rcpf(l);
            const float i1 = ((kq & 1) && hh) ? 0.f : il;
            const auto x0 = __builtin_amdgcn_permlane32_swap(pk(y0[0] * il, y0[1] * il), pk(y1[0] * i1, y1[1] * i1), false, false);
            const auto x1 = __builtin_amdgcn_permlane32_swap(pk(y0[2] * il, y0[3] * il), pk(y1[2] * i1, y1[3] * i1), false, false);
            st16(rWs, u32x4{x0[0], x1[0], x0[1], x1[1]}, so0, 0);
            const float t = ((fabsf(y0[0]) + fabsf(y0[1])) + (fabsf(y0[2]) + fabsf(y0[3]))) + ((fabsf(y1[0]) + fabsf(y1[1])) + (fabsf(y1[2]) + fabsf(y1[3]))) + l;
            if (__any(!(t < 1.0e36f))) *(LAS unsigned*)(uintptr_t)L_BAD = 1;
            if (act3) {
                const unsigned fb = L_A + (unsigned)((8 * hh + ((lane & 15) >> 2)) * 64 + ((lane >> 4) & 1) * 32 + (lane & 3) * 8) + kq * 16 * KB;
#pragma unroll
                for (int i = 0; i < 16; ++i) fa[i] = wfrag<256>(fb + i * KB);
            }
            waitv<0>();
            BAR();
            if (pass == 1 || !__builtin_amdgcn_readfirstlane(*(const volatile LAS unsigned*)(uintptr_t)L_BAD)) break;
            const f32x16 z = {};
            float ma = -INFINITY, mb = -INFINITY;
#pragma unroll 1
            for (int t = 0; t < 16; ++t) {
                const u32x4 k0 = ld16(rK, lo, t * 28672), k1 = ld16(rK, lo + 1024, t * 28672);
                f32x16 ta = MFMA(k0, qa0, z), tb = MFMA(k0, qb0, z); ta = MFMA(k1, qa1, ta); tb = MFMA(k1, qb1, tb);
#pragma unroll
                for (int i = 0; i < 16; ++i) { ma = fmaxf(ma, ta[i]); mb = fmaxf(mb, tb[i]); }
            }
            ma = max32(ma); mb = max32(mb);
            *(LAS float*)(uintptr_t)(L_M + 2048 + ((kq * 2 + qh) * 2 + hh) * 128 + r * 4) = hh ? mb : ma;
            BAR();
#pragma unroll
            for (int i = 0; i < 4; ++i) { ma = fmaxf(ma, *(const LAS float*)(uintptr_t)(L_M + 2048 + ((i * 2 + qh) * 2) * 128 + r * 4)); mb = fmaxf(mb, *(const LAS float*)(uintptr_t)(L_M + 2048 + ((i * 2 + qh) * 2 + 1) * 128 + r * 4)); }
            oa = z; ob = z;
            const unsigned vp = r == DH ? 0x3F803F80u : 0u, vm = r < DH ? ~0u : 0u, vo = (unsigned)(hh * 7168 + (r < DH ? r : 0) * 16);
#pragma unroll 1
            for (int t = 0; t < 16; ++t) {
                const u32x4 k0 = ld16(rK, lo, t * 28672), k1 = ld16(rK, lo + 1024, t * 28672);
                const u32x4 v0 = (ld16(rV, vo, t * 28672) & vm) | vp, v1 = (ld16(rV, vo + 14336, t * 28672) & vm) | vp;
                f32x16 ta = MFMA(k0, qa0, z), tb = MFMA(k0, qb0, z); ta = MFMA(k1, qa1, ta); tb = MFMA(k1, qb1, tb);
                u32x4 p0, p1, p2, p3;
#pragma unroll
                for (int i = 0; i < 4; ++i) {
                    p0[i] = pk(EX2(ta[2 * i] - ma), EX2(ta[2 * i + 1] - ma)); p1[i] = pk(EX2(ta[8 + 2 * i] - ma), EX2(ta[9 + 2 * i] - ma));
                    p2[i] = pk(EX2(tb[2 * i] - mb), EX2(tb[2 * i + 1] - mb)); p3[i] = pk(EX2(tb[8 + 2 * i] - mb), EX2(tb[9 + 2 * i] - mb));
                }
                oa = MFMA(v0, p0, oa); oa = MFMA(v1, p1, oa); ob = MFMA(v0, p2, ob); ob = MFMA(v1, p3, ob);
            }
        }
    }
    }
    const int lane = fresh_lane(), r = lane & 31, hh = lane >> 5;
    const unsigned lo = lane * 16;
    if (tid == 0) set_flag(ws, WS_F2 + qb * 256 + h2 * 8, epoch);
    if (!act3) {
        const int id = ((bi >> 4) * 2 + (bi & 1)) * 8 + xcd;
        const unsigned v2 = (lane >> 2) * 896 + (lane & 3) * 16, trp = (unsigned)((4 * hh + ((lane & 15) >> 2)) * 64 + ((lane >> 4) & 1) * 32 + (lane & 3) * 8);
        const unsigned so = id < 28 ? (unsigned)(((id & 3) * 28 + w) * 16 * 896 + (id >> 2) * 128) : OOB, ld = w * 8 * KB;
#pragma unroll
        for (int i = 0; i < 4; ++i) { dma(rW2, v2, so + i * 128 * 896, ld + 2 * i * KB); dma(rW2, v2, so + i * 128 * 896 + 64, ld + (2 * i + 1) * KB); }
        const unsigned dst = id < 28 ? WS_W2T + (unsigned)((id >> 2) * 224 + (id & 3) * 28 + w) * 1024 : OOB;
        waitv<0>();
#pragma unroll
        for (int i = 0; i < 8; ++i) st16(rWs, wfrag<512>(ld + trp + i * KB), lo, (i >= 6 && w >= 4) ? OOB : dst + ((i & 1) * 112 + (i >> 1) * 8) * 1024);
        waitv<0>();
        BAR();
        if (tid == 0) set_flag(ws, WS_FS + 256 + id * 8, epoch);
        if (b == 14 * 8 && w == 0) {
#pragma unroll 1
            for (int i = 0; i < 4; ++i) wait_flags(ws, WS_F2 + (unsigned)((i * 4 + (lane >> 4)) * 256 + (lane & 15) * 8), epoch);
            if (lane == 0) set_flag(ws, WS_EPOCH, epoch);
        }
        return;
    }

    const int mt = w & 1, kq = w >> 1;
    const unsigned tt0 = (unsigned)(tb3 * 4 + mh * 2), grp = (unsigned)(tb3 * 2 + mh);
    const unsigned xo = ((tt0 + mt) * 32 + r) * 896 + (cb * 64 + 16 * kq + 4 * hh) * 2;
    u32x4 fr[28];
    u32x4 hres;
    f32x4 cs[4];
    {
        const u32x2 xr0 = __builtin_amdgcn_raw_buffer_load_b64(rX, xo, 0, 0), xr1 = __builtin_amdgcn_raw_buffer_load_b64(rX, xo + 16, 0, 0);
#pragma unroll
        for (int g = 0; g < 4; ++g) cs[g] = __builtin_bit_cast(f32x4, ld16(rWs, (8 * g + 4 * hh) * 4, WS_CS1 + (unsigned)(cb * 256 + w * 32) * 4));
#pragma unroll
        for (int i = 0; i < NPK; ++i) fr[i] = fe[i];
#pragma unroll
        for (int i = NPK; i < NW1A; ++i) fr[i] = ld16(rWs, lo, w1b + R4(i) * 1024);
        if (w == 0) wait_flags(ws, WS_F2 + tb3 * 256 + (lane & 15) * 8, epoch);
        BARX();
        u32x4 bf[8];
#pragma unroll
        for (int i = 0; i < 8; ++i) bf[i] = ld16(rWs, lo + (i & 3) * 1024, WS_O + ((tt0 + mt) * 32 + 8 * kq + (i & 4)) * 1024);
        SB();
        f32x16 a0 = {}, a1 = {};
#pragma unroll
        for (int i = 0; i < 8; ++i) { a0 = MFMA(fa[2 * i], bf[i], a0); a1 = MFMA(fa[2 * i + 1], bf[i], a1); }
        PIN(a0); PIN(a1);
        SB();
#pragma unroll
        for (int i = NW1A; i < NW1B; ++i) fr[i] = ld16(rWs, lo, w1b + R4(i) * 1024);
        SB();
        {
            const unsigned dst = L_RED + (unsigned)(mt * 16 + kq) * 2048 + lo;
#pragma unroll
            for (int p = 0; p < 4; ++p) {
                const f32x16& o = (p >> 1) ? a1 : a0;
                *(LAS f32x4*)(uintptr_t)(dst + p * 8192) = f32x4{o[8 * (p & 1)], o[8 * (p & 1) + 1], o[8 * (p & 1) + 2], o[8 * (p & 1) + 3]};
                *(LAS f32x4*)(uintptr_t)(dst + p * 8192 + 1024) = f32x4{o[8 * (p & 1) + 4], o[8 * (p & 1) + 5], o[8 * (p & 1) + 6], o[8 * (p & 1) + 7]};
            }
        }
        float o8[8] = {bflo(xr0[0]), bfhi(xr0[0]), bflo(xr0[1]), bfhi(xr0[1]), bflo(xr1[0]), bfhi(xr1[0]), bflo(xr1[1]), bfhi(xr1[1])};
        unsigned src = L_RED + (unsigned)(mt * 4 + kq) * 8192 + lo;
#pragma unroll
        for (int i = 0; i < 8; ++i) PIN(o8[i]);
        PIN(src);
        BAR();
#pragma unroll
        for (int i = 0; i < 4; ++i) {
            const f32x4 a = lds16f(src + i * 2048), c = lds16f(src + i * 2048 + 1024);
#pragma unroll
            for (int e = 0; e < 4; ++e) { o8[e] += a[e]; o8[4 + e] += c[e]; }
        }
        hres = u32x4{pk(o8[0], o8[1]), pk(o8[2], o8[3]), pk(o8[4], o8[5]), pk(o8[6], o8[7])};
        st16(rWs, hres, lo, WS_H + ((tt0 + mt) * 28 + cb * 4 + kq) * 1024);
        float s1 = 0.f, s2 = 0.f;
#pragma unroll
        for (int e = 0; e < 4; ++e) { const float a = bflo(hres[e]), c = bfhi(hres[e]); s1 += a + c; s2 = fmaf(a, a, fmaf(c, c, s2)); }
        s1 = sum32(s1); s2 = sum32(s2);
        st8(rWs, u32x2{__float_as_uint(s1), __float_as_uint(s2)}, hh ? OOB : r * 8, WS_ST + ((tt0 + mt) * 28 + cb * 4 + kq) * 256);
        waitv<0>();
    }
    float rstd2;
    u32x4 f2[28];
    const int r5 = 2 * ((cb + 2 * g4) % 7);
    const unsigned w2b = WS_W2T + (unsigned)(cb * 2 * 112 + 14 * w) * 1024;
#define R5(t) ((t) + r5 >= 14 ? (t) + r5 - 14 : (t) + r5)
    {
        BARX();
        if (tid == 0) set_flag(ws, WS_F3 + grp * 256 + cb * 8, epoch);
        SB();
#pragma unroll
        for (int i = NW1B; i < 28; ++i) fr[i] = ld16(rWs, lo, w1b + R4(i) * 1024);
#pragma unroll
        for (int i = 0; i < 28; ++i) PIN(fr[i]);
        u32x2 stv[7];
        if (w < 4) {
            if (w == 0) {
                wait_flags(ws, lane < 32 ? WS_F3 + grp * 256 + (unsigned)(lane % 7) * 8 : WS_FS + 256 + (unsigned)(cb * 4 + (lane & 3)) * 8, epoch); *(volatile LAS unsigned*)(uintptr_t)L_GO = 3u;
            } else { while (*(const volatile LAS unsigned*)(uintptr_t)L_GO != 3u) __builtin_amdgcn_s_sleep(1); asm volatile("" ::: "memory"); }
#pragma unroll
            for (int i = 0; i < 14; ++i) dma_sc1(rWs, lo, WS_H + ((tt0 + i / 7) * 28 + R4(4 * (i % 7) + w)) * 1024, L_A + ((i / 7) * 28 + 4 * (i % 7) + w) * KB);
            waitv<7>();
        } else {
            while (*(const volatile LAS unsigned*)(uintptr_t)L_GO != 3u) __builtin_amdgcn_s_sleep(1);
            asm volatile("" ::: "memory");
#pragma unroll
            for (int i = 0; i < 7; ++i) stv[i] = __builtin_amdgcn_raw_buffer_load_b64(rWs, (unsigned)(hh * 256 + r * 8), WS_ST + ((tt0 + (w & 1)) * 28 + 4 * i + (w & 2)) * 256, 16);
        }
        BARX();
        f32x16 a0 = {}, a1 = {};
        u32x4 bq[8];
#pragma unroll
        for (int i = 0; i < PD4; ++i) bq[i] = lds16(L_A + i * KB + lo);
        SB();
        f32x2 sv[2][4];
        float nm[2], ce[2], rs1;
        u32x4 o[2];
        const unsigned hso = WS_H1 + (tt0 * 112 + cb * 16 + w * 2) * 1024;
#define W2LD(q) f2[q] = ld16(rWs, lo, w2b + (unsigned)(((q) & 1) * 112 + R5((q) >> 1)) * 1024)
        auto step = [&](auto sc) __attribute__((always_inline)) {
            constexpr int s = decltype(sc)::value;
            if (s == 16) {
                if (w >= 4) {
                    float s1 = 0.f, s2 = 0.f;
#pragma unroll
                    for (int i = 0; i < 7; ++i) { s1 = add1(s1, __uint_as_float(stv[i][0])); s2 = add1(s2, __uint_as_float(stv[i][1])); }
                    *(LAS f32x2*)(uintptr_t)(L_M + (w - 4) * 512 + lane * 8) = f32x2{s1, s2};
                } else waitv<4>();
                BAR();
            }
            if (s + PD4 < 56) bq[(s + PD4) & 7] = lds16(L_A + (s + PD4) * KB + lo);
            if constexpr (s == 17 || s == 18) {
#pragma unroll
                for (int i = 0; i < 4; ++i) sv[s - 17][i] = *(const LAS f32x2*)(uintptr_t)(L_M + ((i >> 1) * 2 + (s - 17)) * 512 + ((i & 1) * 32 + r) * 8);
            }
            SB();
            if constexpr (s < 28) { a0 = MFMA(fr[s % 28], bq[s & 7], a0); PIN(a0); } else { a1 = MFMA(fr[s % 28], bq[s & 7], a1); PIN(a1); }
            SB();
            if constexpr (s < 28 && s % 4 == 3) W2LD(s / 4);
            if constexpr (s >= 28 && s % 3 == 0 && 7 + (s - 28) / 3 < NW2) W2LD(7 + (s - 28) / 3);
            if constexpr (s == 22 || s == 24) {
                const int m = (s - 22) / 2;
                const float s1 = add1(add1(sv[m][0][0], sv[m][1][0]), add1(sv[m][2][0], sv[m][3][0])), s2 = add1(add1(sv[m][0][1], sv[m][1][1]), add1(sv[m][2][1], sv[m][3][1]));
                const float mean = s1 * (1.f / DM), rs = __builtin_amdgcn_rsqf(fmaxf(s2 * (1.f / DM) - mean * mean, 0.f) + 1e-5f);
                nm[m] = -mean; ce[m] = rs * -1.4426950408889634f;
                if (m == 0) rs1 = rs; else rs1 = mt ? rs : rs1;
                PIN(nm[m]); PIN(ce[m]); PIN(rs1);
            }
            if constexpr (s == 32 || s == 37 || s == 42 || s == 47) {
                const int g = (s - 32) / 5;
                const u32x2 y = silu4(a0[4 * g], a0[4 * g + 1], a0[4 * g + 2], a0[4 * g + 3], cs[g], nm[0], ce[0]);
                o[g >> 1][(g & 1) * 2] = y[0]; o[g >> 1][(g & 1) * 2 + 1] = y[1];
                PIN(o[g >> 1]);
                if (g & 1) st16(rWs, o[g >> 1], lo + (g >> 1) * 1024, hso);
            }
            SB();
        };
        static_for<0, 56>(step);
        rstd2 = rs1;
        asm volatile("s_nop 15" : "+v"(a1));
#pragma unroll
        for (int g = 0; g < 4; ++g) {
            const u32x2 y = silu4(a1[4 * g], a1[4 * g + 1], a1[4 * g + 2], a1[4 * g + 3], cs[g], nm[1], ce[1]);
            o[g >> 1][(g & 1) * 2] = y[0]; o[g >> 1][(g & 1) * 2 + 1] = y[1];
            if (g & 1) st16(rWs, o[g >> 1], lo + (g >> 1) * 1024, hso + 112 * 1024);
        }
        waitv<0>();
    }
    {
        BAR();
        if (tid == 0) set_flag(ws, WS_F4 + grp * 256 + cb * 8, epoch);
        SB();
#pragma unroll
        for (int q = NW2; q < 28; ++q) f2[q] = ld16(rWs, lo, w2b + (unsigned)((q & 1) * 112 + R5(q >> 1)) * 1024);
        if (w == 0) wait_flags(ws, WS_F4 + grp * 256 + (unsigned)(lane % 7) * 8, epoch);
        BARX();
        u32x4 bf[28];
        const unsigned bs = WS_H1 + (tt0 * 112 + 14 * w) * 1024;
#define BLD(t) do { const unsigned o_ = bs + R5(t) * 1024; bf[2 * (t)] = ld16(rWs, lo, o_); bf[2 * (t) + 1] = ld16(rWs, lo, o_ + 112 * 1024); } while (0)
#pragma unroll
        for (int t = 0; t < NB5; ++t) BLD(t);
        SB();
        f32x16 ac[4] = {{}, {}, {}, {}};
#pragma unroll
        for (int t = 0; t < 14; ++t) {
            ac[0] = MFMA(f2[2 * t], bf[2 * t], ac[0]); ac[2] = MFMA(f2[2 * t + 1], bf[2 * t], ac[2]);
            ac[1] = MFMA(f2[2 * t], bf[2 * t + 1], ac[1]); ac[3] = MFMA(f2[2 * t + 1], bf[2 * t + 1], ac[3]);
            PIN(ac[0]); PIN(ac[1]); PIN(ac[2]); PIN(ac[3]);
            SB();
            if (t + NB5 < 14) BLD(t + NB5);
            SB();
        }
#pragma unroll
        for (int p = 0; p < 8; ++p) {
            const f32x16& o = ac[(p >> 2) * 2 + (p & 1)]; const int h8 = ((p >> 1) & 1) * 8;
            const unsigned dst = (unsigned)(p * 8 + w) * 2048 + lo;
            *(LAS f32x4*)(uintptr_t)dst = f32x4{o[h8], o[h8 + 1], o[h8 + 2], o[h8 + 3]};
            *(LAS f32x4*)(uintptr_t)(dst + 1024) = f32x4{o[h8 + 4], o[h8 + 5], o[h8 + 6], o[h8 + 7]};
        }
        BAR();
        f32x4 y0 = {0.f, 0.f, 0.f, 0.f}, y1 = y0;
#pragma unroll
        for (int i = 0; i < 8; ++i) { const unsigned src = (unsigned)(w * 8 + i) * 2048 + lo; y0 += lds16f(src); y1 += lds16f(src + 1024); }
        const u32x2 z0 = {pk(fmaf(y0[0], rstd2, bflo(hres[0])), fmaf(y0[1], rstd2, bfhi(hres[0]))), pk(fmaf(y0[2], rstd2, bflo(hres[1])), fmaf(y0[3], rstd2, bfhi(hres[1])))};
        const u32x2 z1 = {pk(fmaf(y1[0], rstd2, bflo(hres[2])), fmaf(y1[1], rstd2, bfhi(hres[2]))), pk(fmaf(y1[2], rstd2, bflo(hres[3])), fmaf(y1[3], rstd2, bfhi(hres[3])))};
        const auto x0 = __builtin_amdgcn_permlane32_swap(z0[0], z1[0], false, false), x1 = __builtin_amdgcn_permlane32_swap(z0[1], z1[1], false, false);
        __builtin_amdgcn_raw_buffer_store_b128(u32x4{x0[0], x1[0], x0[1], x1[1]}, rOut, xo + hh * 8, 0, 0);
    }
    asm volatile(".Lblk_code_end:");
}

extern "C" void kernel_launch(void* const* d_in, const int* in_sizes, int n_in, void* d_out, int out_size, void* d_ws, size_t ws_size,
                              hipStream_t stream) {
    (void)hipFuncSetAttribute((const void*)block_kernel, hipFuncAttributeMaxDynamicSharedMemorySize, LDS_BYTES);
    block_kernel<<<256, 512, LDS_BYTES, stream>>>(d_in[0], d_in[1], d_in[2], d_in[4], d_in[5], d_out, (char*)d_ws);
}
```
